```python
import torch

def _softmax(x, axis=-1):
    return torch.softmax(x, dim=axis)
def _gelu(x):
    return torch.nn.functional.gelu(x, approximate="tanh")
def _silu(x):
    return torch.nn.functional.silu(x)
def _sigmoid(x):
    return torch.sigmoid(x)
def _layernorm(x, axis=-1, eps=1e-5):
    m = x.mean(dim=axis, keepdim=True)
    v = x.var(dim=axis, keepdim=True, unbiased=False)
    return (x - m) / torch.sqrt(v + eps)
def _scatter_add(tgt, idx, src, axis=0):
    return tgt.clone().index_add_(axis, idx, src)
def _segment_sum(src, seg, n):
    out = torch.zeros((n,) + src.shape[1:], dtype=src.dtype)
    return out.index_add_(0, seg, src)
def _pad(x, pw):
    flat = tuple(v for pair in reversed(pw) for v in pair)
    return torch.nn.functional.pad(x, flat)

kernel_name = 'parallel_sum__grouped_kv_mqa__dense_activation_mish__prompt'

def setup_inputs():

    _w = (lambda a: (lambda t: t.to(torch.bfloat16) if t.is_floating_point() else t)(torch.as_tensor(a)))
    def _A_setup():
        B, S, D, H = 1, 2048, 512, 32
        Dh = D // H
        return {
            "x": _w((lambda *s: torch.randn(*s, dtype=torch.bfloat16) * 0.05)(B, S, D)),
            "wq": _w((lambda *s: torch.randn(*s, dtype=torch.bfloat16) * 0.05)(D, H * Dh)),
            "wk": _w((lambda *s: torch.randn(*s, dtype=torch.bfloat16) * 0.05)(D, Dh)),
            "wv": _w((lambda *s: torch.randn(*s, dtype=torch.bfloat16) * 0.05)(D, Dh)),
            "wo": _w((lambda *s: torch.randn(*s, dtype=torch.bfloat16) * 0.05)(H * Dh, D)),
        }
    _A_in = _A_setup()

    def _B_setup():
        B, S, D, F = 1, 2048, 512, 1792
        return {
            "x": _w((lambda *s: torch.randn(*s, dtype=torch.bfloat16) * 0.05)(B, S, D)),
            "w1": _w((lambda *s: torch.randn(*s, dtype=torch.bfloat16) * 0.8)(D, F) * (D ** -0.5)),
            "b1": _w((lambda *s: torch.randn(*s, dtype=torch.bfloat16) * 0.4)(F)),
            "w2": _w((lambda *s: torch.randn(*s, dtype=torch.bfloat16) * 0.2)(F, D) * (F ** -0.5)),
        }
    _B_in = _B_setup()

    return {
        "A_x": _A_in["x"],
        "A_wq": _A_in["wq"],
        "A_wk": _A_in["wk"],
        "A_wv": _A_in["wv"],
        "A_wo": _A_in["wo"],
        "B_x": _B_in["x"],
        "B_w1": _B_in["w1"],
        "B_b1": _B_in["b1"],
        "B_w2": _B_in["w2"],
    }

def reference(A_x, A_wq, A_wk, A_wv, A_wo, B_x, B_w1, B_b1, B_w2):
    def _A_ref(x, wq, wk, wv, wo):
        B, S, D = x.shape
        H = 32
        Dh = D // H
        q = (lambda x, *d: x.permute(*d))((x @ wq).reshape(B, S, H, Dh), 0, 2, 1, 3)
        k = x @ wk
        v = x @ wv
        scores = torch.einsum("bhqd,bkd->bhqk", q, k) * (Dh ** -0.5)
        p = _softmax(scores, axis=-1)
        o = torch.einsum("bhqk,bkd->bhqd", p, v)
        return (lambda x, *d: x.permute(*d))(o, 0, 2, 1, 3).reshape(B, S, H * Dh) @ wo

    def _B_ref(x, w1, b1, w2):
        h = torch.einsum("bsd,df->bsf", x, w1) + b1
        sp = torch.log1p(torch.exp(-torch.abs(h))) + torch.where(h > 0, h, torch.zeros_like(h))
        act = h * torch.tanh(sp)
        return torch.einsum("bsf,fd->bsd", act, w2)

    x = A_x
    xn = _layernorm(x, axis=-1)
    return x + _A_ref(xn, A_wq, A_wk, A_wv, A_wo) + _B_ref(xn, B_w1, B_b1, B_w2)
```

```cpp
#include <hip/hip_runtime.h>
#include <hip/hip_bf16.h>

typedef unsigned u32;
typedef unsigned long long u64;

struct Buf { const void* p; u32 bytes; u32 fmt; };

__global__ void __launch_bounds__(512, 2) __attribute__((aligned(4096))) fused_mqa_mish(Buf bx, Buf bw1, Buf bwk, Buf bwv, Buf bws, Buf bb1, Buf bwq, Buf bwo, Buf bw2, Buf bout) {
  __shared__ __attribute__((aligned(16))) char smem[163840];
  u64 karg = (u64)__builtin_amdgcn_kernarg_segment_ptr();
  asm volatile(
    "s_load_dwordx16 s[4:19], s[0:1], 0x0\n\t"
    "s_load_dwordx16 s[20:35], s[0:1], 0x40\n\t"
    "s_load_dwordx8 s[36:43], s[0:1], 0x80\n\t"
    "v_readfirstlane_b32 s44, v0\n\t"
    "v_lshrrev_b32 v1, 3, v0\n\t"
    "v_and_b32 v32, 7, v0\n\t"
    "s_and_b32 s60, s2, 7\n\t"
    "s_lshr_b32 s62, s2, 6\n\t"
    "s_lshl2_add_u32 s94, s60, s62\n\t"
    "s_bfe_u32 s45, s2, 0x30003\n\t"
    "s_and_b32 s96, s45, 1\n\t"
    "s_lshl_b32 s60, s96, 5\n\t"
    "s_and_b32 s60, s60, 32\n\t"
    "v_xor_b32 v1, s60, v1\n\t"
    "v_lshlrev_b32 v33, 4, v32\n\t"
    "v_lshl_or_b32 v33, v1, 10, v33\n\t"
    "v_lshl_or_b32 v33, s94, 16, v33\n\t"
    "s_lshr_b32 s98, s44, 8\n\t"
    "s_lshr_b32 s44, s44, 6\n\t"
    "s_mul_i32 s95, s94, 2\n\t"
    "s_add_u32 s49, s44, s98\n\t"
    "s_and_b32 s49, s49, 3\n\t"
    "s_lshl2_add_u32 s44, s98, s49\n\t"
    "s_and_b32 s95, s95, 31\n\t"
    "s_and_b32 s60, s2, 0xf8\n\t"
    "s_add_u32 s60, s60, s44\n\t"
    "s_getpc_b64 s[64:65]\n\t"
    ".Lfmm_begin:\n\t"
    "s_lshl_b32 s60, s60, 7\n\t"
    "s_min_u32 s62, s60, .Lfmm_end-.Lfmm_begin-4\n\t"
    "s_add_u32 s63, s60, 64\n\t"
    "s_min_u32 s63, s63, .Lfmm_end-.Lfmm_begin-4\n\t"
    "v_lshrrev_b32 v53, 1, v32\n\t"
    "v_subrev_u32 v53, s95, v53\n\t"
    "v_and_b32 v54, 1, v0\n\t"
    "v_and_b32 v55, 3, v53\n\t"
    "v_lshl_or_b32 v55, v54, 2, v55\n\t"
    "v_and_b32 v56, 31, v1\n\t"
    "v_xor_b32 v55, v55, v56\n\t"
    "v_lshlrev_b32 v55, 4, v55\n\t"
    "v_lshl_or_b32 v55, v54, 9, v55\n\t"
    "v_and_b32 v56, 32, v1\n\t"
    "v_lshl_or_b32 v55, v56, 5, v55\n\t"
    "v_lshl_or_b32 v55, v53, 11, v55\n\t"
    "v_and_b32 v196, 0xffff, v55\n\t"
    "v_add_u32 v55, 0x2000, v55\n\t"
    "v_and_b32 v197, 0xffff, v55\n\t"
    "v_add_u32 v55, 0x2000, v55\n\t"
    "v_and_b32 v198, 0xffff, v55\n\t"
    "v_add_u32 v55, 0x2000, v55\n\t"
    "v_and_b32 v199, 0xffff, v55\n\t"
    "v_add_u32 v55, 0x2000, v55\n\t"
    "v_and_b32 v200, 0xffff, v55\n\t"
    "v_add_u32 v55, 0x2000, v55\n\t"
    "v_and_b32 v201, 0xffff, v55\n\t"
    "v_add_u32 v55, 0x2000, v55\n\t"
    "v_and_b32 v202, 0xffff, v55\n\t"
    "v_add_u32 v55, 0x2000, v55\n\t"
    "v_and_b32 v203, 0xffff, v55\n\t"
    "s_lshl_b32 s46, s44, 11\n\t"
    "s_lshl_b32 s69, s49, 11\n\t"
    "s_add_u32 s61, s95, s49\n\t"
    "s_mul_i32 s97, s45, 448\n\t"
    "s_lshl_b32 s61, s61, 4\n\t"
    "s_setprio 1\n\t"
    "s_cmp_lg_u32 s98, 0\n\t"
    "s_waitcnt lgkmcnt(0)\n\t"
    "s_cbranch_scc1 1f\n\t"
    "buffer_load_dwordx4 v[100:103], v33, s[4:7], 0 offen offset:0\n\t"
    "buffer_load_dwordx4 v[104:107], v33, s[4:7], 0 offen offset:128\n\t"
    "buffer_load_dwordx4 v[108:111], v33, s[4:7], 0 offen offset:256\n\t"
    "buffer_load_dwordx4 v[112:115], v33, s[4:7], 0 offen offset:384\n\t"
    "buffer_load_dwordx4 v[116:119], v33, s[4:7], 0 offen offset:512\n\t"
    "buffer_load_dwordx4 v[120:123], v33, s[4:7], 0 offen offset:640\n\t"
    "buffer_load_dwordx4 v[124:127], v33, s[4:7], 0 offen offset:768\n\t"
    "buffer_load_dwordx4 v[128:131], v33, s[4:7], 0 offen offset:896\n\t"
    "s_setprio 0\n\t"
    "1:\n\t"
    "s_load_dwordx2 s[66:67], s[20:21], 133120\n\t"
    "s_load_dword s3, s[64:65], s62\n\t"
    "s_load_dword s3, s[64:65], s63\n\t"
    "s_and_b32 s48, s97, 0xffffff80\n\t"
    "s_lshl_b32 s60, s96, 8\n\t"
    "s_sub_u32 s47, s48, s60\n\t"
    "s_add_u32 s48, s48, s60\n\t"
    "s_add_u32 s47, s47, 256\n\t"
    "v_and_b32 v34, 63, v0\n\t"
    "v_bfe_u32 v35, v0, 5, 1\n\t"
    "v_and_b32 v36, 3, v0\n\t"
    "v_bfe_u32 v37, v0, 2, 3\n\t"
    "v_lshlrev_b32 v38, 4, v36\n\t"
    "v_lshl_or_b32 v38, v35, 6, v38\n\t"
    "v_lshlrev_b32 v4, 4, v34\n\t"
    "v_bfe_u32 v44, v0, 2, 2\n\t"
    "v_bfe_u32 v45, v0, 4, 2\n\t"
    "v_lshl_or_b32 v47, v45, 2, v36\n\t"
    "v_mul_u32_u24 v236, 224, v44\n\t"
    "v_add_lshl_u32 v236, v236, v47, 4\n\t"
    "v_add_u32 v237, 13312, v236\n\t"
    "v_add_u32 v238, 13312, v237\n\t"
    "v_add_u32 v239, 13312, v238\n\t"
    "s_lshl_b32 s68, s49, 12\n\t"
    "s_lshl2_add_u32 s60, s49, s95\n\t"
    "s_lshl_b32 s60, s60, 9\n\t"
    "v_add_u32 v60, s60, v4\n\t"
    "v_add_u32 v61, 0x400, v60\n\t"
    "v_and_b32 v60, 0x3fff, v60\n\t"
    "v_and_b32 v61, 0x3fff, v61\n\t"
    "v_xor_b32 v62, 0x2000, v60\n\t"
    "v_xor_b32 v63, 0x2000, v61\n\t"
    "s_cmp_lt_u32 s45, 4\n\t"
    "s_cselect_b32 s12, s12, s16\n\t"
    "s_cselect_b32 s13, s13, s17\n\t"
    "v_and_b32 v46, 16, v0\n\t"
    "v_and_b32 v48, 15, v0\n\t"
    "v_bfrev_b32 v49, v44\n\t"
    "v_lshrrev_b32 v49, 28, v49\n\t"
    "v_or_b32 v49, v49, v36\n\t"
    "v_or_b32 v50, v49, v46\n\t"
    "v_lshlrev_b32 v50, 1, v50\n\t"
    "s_lshl_b32 s62, s96, 2\n\t"
    "s_sub_u32 s62, s49, s62\n\t"
    "s_add_u32 s16, s62, 4\n\t"
    "s_mul_i32 s63, s96, 3\n\t"
    "s_add_u32 s17, s63, s49\n\t"
    "s_lshl_b32 s64, s16, 6\n\t"
    "s_lshl_b32 s65, s17, 6\n\t"
    "s_add_u32 s64, s64, s97\n\t"
    "s_add_u32 s65, s65, s97\n\t"
    "buffer_load_ushort v28, v50, s[24:27], s64 offen\n\t"
    "buffer_load_ushort v30, v50, s[24:27], s65 offen\n\t"
    "s_and_b32 s60, s44, 2\n\t"
    "v_and_b32 v41, 31, v0\n\t"
    "v_lshl_or_b32 v42, v35, 2, s60\n\t"
    "v_xor_b32 v42, v42, v41\n\t"
    "v_lshlrev_b32 v42, 4, v42\n\t"
    "v_lshl_or_b32 v42, v35, 9, v42\n\t"
    "s_lshl1_add_u32 s62, s60, s98\n\t"
    "s_add_u32 s62, s62, 32\n\t"
    "v_lshl_add_u32 v16, s62, 10, v42\n\t"
    "v_xor_b32 v17, 0x810, v16\n\t"
    "v_xor_b32 v18, 0x1020, v16\n\t"
    "v_xor_b32 v19, 0x1830, v16\n\t"
    "v_bfrev_b32 v43, v0\n\t"
    "v_lshrrev_b32 v43, 30, v43\n\t"
    "v_lshlrev_b32 v26, 3, v43\n\t"
    "v_lshl_or_b32 v26, v44, 6, v26\n\t"
    "v_lshl_or_b32 v26, v46, 1, v26\n\t"
    "v_lshl_or_b32 v47, v35, 11, v26\n\t"
    "s_add_u32 s64, s49, s96\n\t"
    "s_lshl4_add_u32 s64, s60, s64\n\t"
    "v_lshl_add_u32 v96, s64, 8, v47\n\t"
    "v_xor_b32 v97, 0x2000, v96\n\t"
    "v_bfe_u32 v51, v0, 4, 1\n\t"
    "v_lshl_or_b32 v52, s96, 4, v48\n\t"
    "v_lshl_or_b32 v53, v35, 5, v52\n\t"
    "v_lshl_or_b32 v54, v35, 4, v49\n\t"
    "s_lshl_b32 s62, s96, 5\n\t"
    "s_add_u32 s62, s62, 64\n\t"
    "v_add_u32 v54, s62, v54\n\t"
    "s_cmp_lt_u32 s45, 4\n\t"
    "s_cselect_b64 vcc, -1, 0\n\t"
    "s_mov_b32 s50, 0x3f000000\n\t"
    "s_cselect_b32 s50, 0x3f317218, s50\n\t"
    "v_cndmask_b32 v53, v54, v53, vcc\n\t"
    "v_lshlrev_b32 v53, 1, v53\n\t"
    "v_add_lshl_u32 v21, v53, v51, 3\n\t"
    "v_add_u32 v25, 147456, v4\n\t"
    "s_add_u32 s62, s46, 147456\n\t"
    "v_add_u32 v20, s62, v4\n\t"
    "v_lshl_or_b32 v53, v45, 5, v48\n\t"
    "s_lshl_b32 s62, s98, 13\n\t"
    "s_add_u32 s62, s62, 147456\n\t"
    "v_lshl_add_u32 v22, v53, 3, s62\n\t"
    "v_lshl_or_b32 v53, v51, 2, v35\n\t"
    "v_xor_b32 v53, v53, v52\n\t"
    "v_lshlrev_b32 v53, 4, v53\n\t"
    "v_lshl_or_b32 v53, v51, 9, v53\n\t"
    "v_lshl_or_b32 v53, v35, 11, v53\n\t"
    "s_bfe_u32 s62, s45, 0x10001\n\t"
    "s_lshl1_add_u32 s18, s94, s62\n\t"
    "s_lshl_b32 s63, s98, 5\n\t"
    "s_add_u32 s62, s62, s63\n\t"
    "s_lshl_b32 s18, s18, 11\n\t"
    "s_add_u32 s62, s62, 32\n\t"
    "v_lshl_add_u32 v23, s62, 10, v53\n\t"
    "v_xor_b32 v24, 32, v23\n\t"
    "v_add_u32 v53, -1, v35\n\t"
    "v_and_b32 v2, 0xbaac3f39, v53\n\t"
    "v_and_b32 v3, 0x366d, v53\n\t"
    "v_lshl_or_b32 v27, v37, 10, v38\n\t"
    "s_cmp_lt_u32 s44, 4\n\t"
    "s_cbranch_scc1 2f\n\t"
    "buffer_load_dwordx4 v[100:103], v33, s[4:7], 0 offen offset:0\n\t"
    "buffer_load_dwordx4 v[104:107], v33, s[4:7], 0 offen offset:128\n\t"
    "buffer_load_dwordx4 v[108:111], v33, s[4:7], 0 offen offset:256\n\t"
    "buffer_load_dwordx4 v[112:115], v33, s[4:7], 0 offen offset:384\n\t"
    "buffer_load_dwordx4 v[116:119], v33, s[4:7], 0 offen offset:512\n\t"
    "buffer_load_dwordx4 v[120:123], v33, s[4:7], 0 offen offset:640\n\t"
    "buffer_load_dwordx4 v[124:127], v33, s[4:7], 0 offen offset:768\n\t"
    "buffer_load_dwordx4 v[128:131], v33, s[4:7], 0 offen offset:896\n\t"
    "s_branch .Lfmm_lnb\n\t"
    "2:\n\t"
    "s_waitcnt vmcnt(9)\n\t"
    "v_lshlrev_b32 v132, 16, v100\n\t"
    "v_and_b32 v133, 0xffff0000, v100\n\t"
    "v_lshlrev_b32 v134, 16, v101\n\t"
    "v_and_b32 v135, 0xffff0000, v101\n\t"
    "v_lshlrev_b32 v136, 16, v102\n\t"
    "v_and_b32 v137, 0xffff0000, v102\n\t"
    "v_lshlrev_b32 v138, 16, v103\n\t"
    "v_and_b32 v139, 0xffff0000, v103\n\t"
    "v_pk_add_f32 v[204:205], v[132:133], v[136:137]\n\t"
    "v_pk_mul_f32 v[208:209], v[132:133], v[132:133]\n\t"
    "v_pk_add_f32 v[206:207], v[134:135], v[138:139]\n\t"
    "v_pk_mul_f32 v[210:211], v[134:135], v[134:135]\n\t"
    "v_pk_fma_f32 v[208:209], v[136:137], v[136:137], v[208:209]\n\t"
    "v_pk_fma_f32 v[210:211], v[138:139], v[138:139], v[210:211]\n\t"
    "s_waitcnt vmcnt(8)\n\t"
    "v_lshlrev_b32 v140, 16, v104\n\t"
    "v_and_b32 v141, 0xffff0000, v104\n\t"
    "v_lshlrev_b32 v142, 16, v105\n\t"
    "v_and_b32 v143, 0xffff0000, v105\n\t"
    "v_lshlrev_b32 v144, 16, v106\n\t"
    "v_and_b32 v145, 0xffff0000, v106\n\t"
    "v_lshlrev_b32 v146, 16, v107\n\t"
    "v_and_b32 v147, 0xffff0000, v107\n\t"
    "v_pk_add_f32 v[204:205], v[204:205], v[140:141]\n\t"
    "v_pk_fma_f32 v[208:209], v[140:141], v[140:141], v[208:209]\n\t"
    "v_pk_add_f32 v[206:207], v[206:207], v[142:143]\n\t"
    "v_pk_fma_f32 v[210:211], v[142:143], v[142:143], v[210:211]\n\t"
    "v_pk_add_f32 v[204:205], v[204:205], v[144:145]\n\t"
    "v_pk_fma_f32 v[208:209], v[144:145], v[144:145], v[208:209]\n\t"
    "v_pk_add_f32 v[206:207], v[206:207], v[146:147]\n\t"
    "v_pk_fma_f32 v[210:211], v[146:147], v[146:147], v[210:211]\n\t"
    "s_waitcnt vmcnt(7)\n\t"
    "v_lshlrev_b32 v148, 16, v108\n\t"
    "v_and_b32 v149, 0xffff0000, v108\n\t"
    "v_lshlrev_b32 v150, 16, v109\n\t"
    "v_and_b32 v151, 0xffff0000, v109\n\t"
    "v_lshlrev_b32 v152, 16, v110\n\t"
    "v_and_b32 v153, 0xffff0000, v110\n\t"
    "v_lshlrev_b32 v154, 16, v111\n\t"
    "v_and_b32 v155, 0xffff0000, v111\n\t"
    "v_pk_add_f32 v[204:205], v[204:205], v[148:149]\n\t"
    "v_pk_fma_f32 v[208:209], v[148:149], v[148:149], v[208:209]\n\t"
    "v_pk_add_f32 v[206:207], v[206:207], v[150:151]\n\t"
    "v_pk_fma_f32 v[210:211], v[150:151], v[150:151], v[210:211]\n\t"
    "v_pk_add_f32 v[204:205], v[204:205], v[152:153]\n\t"
    "v_pk_fma_f32 v[208:209], v[152:153], v[152:153], v[208:209]\n\t"
    "v_pk_add_f32 v[206:207], v[206:207], v[154:155]\n\t"
    "v_pk_fma_f32 v[210:211], v[154:155], v[154:155], v[210:211]\n\t"
    "s_waitcnt vmcnt(6)\n\t"
    "v_lshlrev_b32 v156, 16, v112\n\t"
    "v_and_b32 v157, 0xffff0000, v112\n\t"
    "v_lshlrev_b32 v158, 16, v113\n\t"
    "v_and_b32 v159, 0xffff0000, v113\n\t"
    "v_lshlrev_b32 v160, 16, v114\n\t"
    "v_and_b32 v161, 0xffff0000, v114\n\t"
    "v_lshlrev_b32 v162, 16, v115\n\t"
    "v_and_b32 v163, 0xffff0000, v115\n\t"
    "v_pk_add_f32 v[204:205], v[204:205], v[156:157]\n\t"
    "v_pk_fma_f32 v[208:209], v[156:157], v[156:157], v[208:209]\n\t"
    "v_pk_add_f32 v[206:207], v[206:207], v[158:159]\n\t"
    "v_pk_fma_f32 v[210:211], v[158:159], v[158:159], v[210:211]\n\t"
    "v_pk_add_f32 v[204:205], v[204:205], v[160:161]\n\t"
    "v_pk_fma_f32 v[208:209], v[160:161], v[160:161], v[208:209]\n\t"
    "v_pk_add_f32 v[206:207], v[206:207], v[162:163]\n\t"
    "v_pk_fma_f32 v[210:211], v[162:163], v[162:163], v[210:211]\n\t"
    "s_waitcnt vmcnt(5)\n\t"
    "v_lshlrev_b32 v164, 16, v116\n\t"
    "v_and_b32 v165, 0xffff0000, v116\n\t"
    "v_lshlrev_b32 v166, 16, v117\n\t"
    "v_and_b32 v167, 0xffff0000, v117\n\t"
    "v_lshlrev_b32 v168, 16, v118\n\t"
    "v_and_b32 v169, 0xffff0000, v118\n\t"
    "v_lshlrev_b32 v170, 16, v119\n\t"
    "v_and_b32 v171, 0xffff0000, v119\n\t"
    "v_pk_add_f32 v[204:205], v[204:205], v[164:165]\n\t"
    "v_pk_fma_f32 v[208:209], v[164:165], v[164:165], v[208:209]\n\t"
    "v_pk_add_f32 v[206:207], v[206:207], v[166:167]\n\t"
    "v_pk_fma_f32 v[210:211], v[166:167], v[166:167], v[210:211]\n\t"
    "v_pk_add_f32 v[204:205], v[204:205], v[168:169]\n\t"
    "v_pk_fma_f32 v[208:209], v[168:169], v[168:169], v[208:209]\n\t"
    "v_pk_add_f32 v[206:207], v[206:207], v[170:171]\n\t"
    "v_pk_fma_f32 v[210:211], v[170:171], v[170:171], v[210:211]\n\t"
    "s_waitcnt vmcnt(4)\n\t"
    "v_lshlrev_b32 v172, 16, v120\n\t"
    "v_and_b32 v173, 0xffff0000, v120\n\t"
    "v_lshlrev_b32 v174, 16, v121\n\t"
    "v_and_b32 v175, 0xffff0000, v121\n\t"
    "v_lshlrev_b32 v176, 16, v122\n\t"
    "v_and_b32 v177, 0xffff0000, v122\n\t"
    "v_lshlrev_b32 v178, 16, v123\n\t"
    "v_and_b32 v179, 0xffff0000, v123\n\t"
    "v_pk_add_f32 v[204:205], v[204:205], v[172:173]\n\t"
    "v_pk_fma_f32 v[208:209], v[172:173], v[172:173], v[208:209]\n\t"
    "v_pk_add_f32 v[206:207], v[206:207], v[174:175]\n\t"
    "v_pk_fma_f32 v[210:211], v[174:175], v[174:175], v[210:211]\n\t"
    "v_pk_add_f32 v[204:205], v[204:205], v[176:177]\n\t"
    "v_pk_fma_f32 v[208:209], v[176:177], v[176:177], v[208:209]\n\t"
    "v_pk_add_f32 v[206:207], v[206:207], v[178:179]\n\t"
    "v_pk_fma_f32 v[210:211], v[178:179], v[178:179], v[210:211]\n\t"
    "s_waitcnt vmcnt(3)\n\t"
    "v_lshlrev_b32 v180, 16, v124\n\t"
    "v_and_b32 v181, 0xffff0000, v124\n\t"
    "v_lshlrev_b32 v182, 16, v125\n\t"
    "v_and_b32 v183, 0xffff0000, v125\n\t"
    "v_lshlrev_b32 v184, 16, v126\n\t"
    "v_and_b32 v185, 0xffff0000, v126\n\t"
    "v_lshlrev_b32 v186, 16, v127\n\t"
    "v_and_b32 v187, 0xffff0000, v127\n\t"
    "v_pk_add_f32 v[204:205], v[204:205], v[180:181]\n\t"
    "v_pk_fma_f32 v[208:209], v[180:181], v[180:181], v[208:209]\n\t"
    "v_pk_add_f32 v[206:207], v[206:207], v[182:183]\n\t"
    "v_pk_fma_f32 v[210:211], v[182:183], v[182:183], v[210:211]\n\t"
    "v_pk_add_f32 v[204:205], v[204:205], v[184:185]\n\t"
    "v_pk_fma_f32 v[208:209], v[184:185], v[184:185], v[208:209]\n\t"
    "v_pk_add_f32 v[206:207], v[206:207], v[186:187]\n\t"
    "v_pk_fma_f32 v[210:211], v[186:187], v[186:187], v[210:211]\n\t"
    "s_waitcnt vmcnt(2)\n\t"
    "v_lshlrev_b32 v188, 16, v128\n\t"
    "v_and_b32 v189, 0xffff0000, v128\n\t"
    "v_lshlrev_b32 v190, 16, v129\n\t"
    "v_and_b32 v191, 0xffff0000, v129\n\t"
    "v_lshlrev_b32 v192, 16, v130\n\t"
    "v_and_b32 v193, 0xffff0000, v130\n\t"
    "v_lshlrev_b32 v194, 16, v131\n\t"
    "v_and_b32 v195, 0xffff0000, v131\n\t"
    "v_pk_add_f32 v[204:205], v[204:205], v[188:189]\n\t"
    "v_pk_fma_f32 v[208:209], v[188:189], v[188:189], v[208:209]\n\t"
    "v_pk_add_f32 v[206:207], v[206:207], v[190:191]\n\t"
    "v_pk_fma_f32 v[210:211], v[190:191], v[190:191], v[210:211]\n\t"
    "v_pk_add_f32 v[204:205], v[204:205], v[192:193]\n\t"
    "v_pk_fma_f32 v[208:209], v[192:193], v[192:193], v[208:209]\n\t"
    "v_pk_add_f32 v[206:207], v[206:207], v[194:195]\n\t"
    "v_pk_fma_f32 v[210:211], v[194:195], v[194:195], v[210:211]\n\t"
    "v_pk_add_f32 v[204:205], v[204:205], v[206:207]\n\t"
    "v_pk_add_f32 v[208:209], v[208:209], v[210:211]\n\t"
    "v_add_f32 v204, v204, v205\n\t"
    "v_add_f32 v208, v208, v209\n\t"
    "s_nop 0\n\t"
    "v_add_f32_dpp v204, v204, v204 quad_perm:[1,0,3,2] row_mask:0xf bank_mask:0xf bound_ctrl:1\n\t"
    "v_add_f32_dpp v208, v208, v208 quad_perm:[1,0,3,2] row_mask:0xf bank_mask:0xf bound_ctrl:1\n\t"
    "s_nop 0\n\t"
    "v_add_f32_dpp v204, v204, v204 quad_perm:[2,3,0,1] row_mask:0xf bank_mask:0xf bound_ctrl:1\n\t"
    "v_add_f32_dpp v208, v208, v208 quad_perm:[2,3,0,1] row_mask:0xf bank_mask:0xf bound_ctrl:1\n\t"
    "s_nop 0\n\t"
    "v_add_f32_dpp v204, v204, v204 row_half_mirror row_mask:0xf bank_mask:0xf bound_ctrl:1\n\t"
    "v_add_f32_dpp v208, v208, v208 row_half_mirror row_mask:0xf bank_mask:0xf bound_ctrl:1\n\t"
    "v_mul_f32 v204, 0x3b000000, v204\n\t"
    "v_mul_f32 v208, 0x3b000000, v208\n\t"
    "v_fma_f32 v208, -v204, v204, v208\n\t"
    "v_max_f32 v208, 0, v208\n\t"
    "v_add_f32 v208, 0x3727c5ac, v208\n\t"
    "v_rsq_f32 v208, v208\n\t"
    "s_nop 0\n\t"
    "v_mul_f32 v208, 0x3f38aa3b, v208\n\t"
    "v_mul_f32 v210, -v204, v208\n\t"
    "v_pk_fma_f32 v[132:133], v[132:133], v[208:209], v[210:211] op_sel_hi:[1,0,0]\n\t"
    "v_pk_fma_f32 v[134:135], v[134:135], v[208:209], v[210:211] op_sel_hi:[1,0,0]\n\t"
    "v_pk_fma_f32 v[136:137], v[136:137], v[208:209], v[210:211] op_sel_hi:[1,0,0]\n\t"
    "v_pk_fma_f32 v[138:139], v[138:139], v[208:209], v[210:211] op_sel_hi:[1,0,0]\n\t"
    "v_cvt_pk_bf16_f32 v100, v132, v133\n\t"
    "v_cvt_pk_bf16_f32 v101, v134, v135\n\t"
    "v_cvt_pk_bf16_f32 v102, v136, v137\n\t"
    "v_cvt_pk_bf16_f32 v103, v138, v139\n\t"
    "ds_write_b128 v196, v[100:103] offset:32768\n\t"
    "v_pk_fma_f32 v[140:141], v[140:141], v[208:209], v[210:211] op_sel_hi:[1,0,0]\n\t"
    "v_pk_fma_f32 v[142:143], v[142:143], v[208:209], v[210:211] op_sel_hi:[1,0,0]\n\t"
    "v_pk_fma_f32 v[144:145], v[144:145], v[208:209], v[210:211] op_sel_hi:[1,0,0]\n\t"
    "v_pk_fma_f32 v[146:147], v[146:147], v[208:209], v[210:211] op_sel_hi:[1,0,0]\n\t"
    "v_cvt_pk_bf16_f32 v104, v140, v141\n\t"
    "v_cvt_pk_bf16_f32 v105, v142, v143\n\t"
    "v_cvt_pk_bf16_f32 v106, v144, v145\n\t"
    "v_cvt_pk_bf16_f32 v107, v146, v147\n\t"
    "ds_write_b128 v197, v[104:107] offset:32768\n\t"
    "v_pk_fma_f32 v[148:149], v[148:149], v[208:209], v[210:211] op_sel_hi:[1,0,0]\n\t"
    "v_pk_fma_f32 v[150:151], v[150:151], v[208:209], v[210:211] op_sel_hi:[1,0,0]\n\t"
    "v_pk_fma_f32 v[152:153], v[152:153], v[208:209], v[210:211] op_sel_hi:[1,0,0]\n\t"
    "v_pk_fma_f32 v[154:155], v[154:155], v[208:209], v[210:211] op_sel_hi:[1,0,0]\n\t"
    "v_cvt_pk_bf16_f32 v108, v148, v149\n\t"
    "v_cvt_pk_bf16_f32 v109, v150, v151\n\t"
    "v_cvt_pk_bf16_f32 v110, v152, v153\n\t"
    "v_cvt_pk_bf16_f32 v111, v154, v155\n\t"
    "ds_write_b128 v198, v[108:111] offset:32768\n\t"
    "v_pk_fma_f32 v[156:157], v[156:157], v[208:209], v[210:211] op_sel_hi:[1,0,0]\n\t"
    "v_pk_fma_f32 v[158:159], v[158:159], v[208:209], v[210:211] op_sel_hi:[1,0,0]\n\t"
    "v_pk_fma_f32 v[160:161], v[160:161], v[208:209], v[210:211] op_sel_hi:[1,0,0]\n\t"
    "v_pk_fma_f32 v[162:163], v[162:163], v[208:209], v[210:211] op_sel_hi:[1,0,0]\n\t"
    "v_cvt_pk_bf16_f32 v112, v156, v157\n\t"
    "v_cvt_pk_bf16_f32 v113, v158, v159\n\t"
    "v_cvt_pk_bf16_f32 v114, v160, v161\n\t"
    "v_cvt_pk_bf16_f32 v115, v162, v163\n\t"
    "ds_write_b128 v199, v[112:115] offset:32768\n\t"
    "v_pk_fma_f32 v[164:165], v[164:165], v[208:209], v[210:211] op_sel_hi:[1,0,0]\n\t"
    "v_pk_fma_f32 v[166:167], v[166:167], v[208:209], v[210:211] op_sel_hi:[1,0,0]\n\t"
    "v_pk_fma_f32 v[168:169], v[168:169], v[208:209], v[210:211] op_sel_hi:[1,0,0]\n\t"
    "v_pk_fma_f32 v[170:171], v[170:171], v[208:209], v[210:211] op_sel_hi:[1,0,0]\n\t"
    "v_cvt_pk_bf16_f32 v116, v164, v165\n\t"
    "v_cvt_pk_bf16_f32 v117, v166, v167\n\t"
    "v_cvt_pk_bf16_f32 v118, v168, v169\n\t"
    "v_cvt_pk_bf16_f32 v119, v170, v171\n\t"
    "ds_write_b128 v200, v[116:119] offset:32768\n\t"
    "v_pk_fma_f32 v[172:173], v[172:173], v[208:209], v[210:211] op_sel_hi:[1,0,0]\n\t"
    "v_pk_fma_f32 v[174:175], v[174:175], v[208:209], v[210:211] op_sel_hi:[1,0,0]\n\t"
    "v_pk_fma_f32 v[176:177], v[176:177], v[208:209], v[210:211] op_sel_hi:[1,0,0]\n\t"
    "v_pk_fma_f32 v[178:179], v[178:179], v[208:209], v[210:211] op_sel_hi:[1,0,0]\n\t"
    "v_cvt_pk_bf16_f32 v120, v172, v173\n\t"
    "v_cvt_pk_bf16_f32 v121, v174, v175\n\t"
    "v_cvt_pk_bf16_f32 v122, v176, v177\n\t"
    "v_cvt_pk_bf16_f32 v123, v178, v179\n\t"
    "ds_write_b128 v201, v[120:123] offset:32768\n\t"
    "v_pk_fma_f32 v[180:181], v[180:181], v[208:209], v[210:211] op_sel_hi:[1,0,0]\n\t"
    "v_pk_fma_f32 v[182:183], v[182:183], v[208:209], v[210:211] op_sel_hi:[1,0,0]\n\t"
    "v_pk_fma_f32 v[184:185], v[184:185], v[208:209], v[210:211] op_sel_hi:[1,0,0]\n\t"
    "v_pk_fma_f32 v[186:187], v[186:187], v[208:209], v[210:211] op_sel_hi:[1,0,0]\n\t"
    "v_cvt_pk_bf16_f32 v124, v180, v181\n\t"
    "v_cvt_pk_bf16_f32 v125, v182, v183\n\t"
    "v_cvt_pk_bf16_f32 v126, v184, v185\n\t"
    "v_cvt_pk_bf16_f32 v127, v186, v187\n\t"
    "ds_write_b128 v202, v[124:127] offset:32768\n\t"
    "v_pk_fma_f32 v[188:189], v[188:189], v[208:209], v[210:211] op_sel_hi:[1,0,0]\n\t"
    "v_pk_fma_f32 v[190:191], v[190:191], v[208:209], v[210:211] op_sel_hi:[1,0,0]\n\t"
    "v_pk_fma_f32 v[192:193], v[192:193], v[208:209], v[210:211] op_sel_hi:[1,0,0]\n\t"
    "v_pk_fma_f32 v[194:195], v[194:195], v[208:209], v[210:211] op_sel_hi:[1,0,0]\n\t"
    "v_cvt_pk_bf16_f32 v128, v188, v189\n\t"
    "v_cvt_pk_bf16_f32 v129, v190, v191\n\t"
    "v_cvt_pk_bf16_f32 v130, v192, v193\n\t"
    "v_cvt_pk_bf16_f32 v131, v194, v195\n\t"
    "ds_write_b128 v203, v[128:131] offset:32768\n\t"
    "s_add_u32 s60, s61, 64\n\t"
    "s_and_b32 s60, s60, 511\n\t"
    "s_mul_i32 s60, s60, 3584\n\t"
    "s_add_u32 m0, s68, 16384\n\t"
    "s_add_u32 s60, s60, s47\n\t"
    "buffer_load_dwordx4 v236, s[8:11], s60 offen offset:0 lds\n\t"
    "buffer_load_dwordx4 v237, s[8:11], s60 offen offset:1024 lds\n\t"
    "buffer_load_dwordx4 v238, s[8:11], s60 offen offset:2048 lds\n\t"
    "buffer_load_dwordx4 v239, s[8:11], s60 offen offset:3072 lds\n\t"
    "s_add_u32 s60, s61, 128\n\t"
    "s_and_b32 s60, s60, 511\n\t"
    "s_mul_i32 s60, s60, 3584\n\t"
    "s_add_u32 m0, s68, 98304\n\t"
    "s_add_u32 s60, s60, s47\n\t"
    "buffer_load_dwordx4 v236, s[8:11], s60 offen offset:0 lds\n\t"
    "buffer_load_dwordx4 v237, s[8:11], s60 offen offset:1024 lds\n\t"
    "buffer_load_dwordx4 v238, s[8:11], s60 offen offset:2048 lds\n\t"
    "buffer_load_dwordx4 v239, s[8:11], s60 offen offset:3072 lds\n\t"
    "s_getpc_b64 s[62:63]\n\t"
    ".Lfmm_here:\n\t"
    "s_add_u32 s60, s68, .Lfmm_page1-.Lfmm_here\n\t"
    "s_add_u32 s62, s62, s60\n\t"
    "s_addc_u32 s63, s63, 0\n\t"
    "s_mov_b64 exec, 0\n\t"
    "s_swappc_b64 s[64:65], s[62:63]\n\t"
    "s_cmp_ge_u32 s49, 2\n\t"
    "s_cbranch_scc1 3f\n\t"
    "s_add_u32 s62, s62, 0x4000\n\t"
    "s_addc_u32 s63, s63, 0\n\t"
    "s_swappc_b64 s[64:65], s[62:63]\n\t"
    "3:\n\t"
    "s_mov_b64 exec, -1\n\t"
    "s_waitcnt vmcnt(8)\n\t"
    "s_add_u32 s15, s61, 464\n\t"
    "s_add_u32 s61, s61, 464\n\t"
    "s_movk_i32 s69, 0\n\t"
    "s_branch .Lfmm_lnend\n\t"
    ".Lfmm_ret:\n\t"
    "s_setpc_b64 s[64:65]\n\t"
    ".Lfmm_sd0:\n\t"
    "s_waitcnt lgkmcnt(9)\n\t"
    "v_pk_add_f32 v[228:229], v[228:229], v[232:233]\n\t"
    "v_pk_add_f32 v[230:231], v[230:231], v[234:235]\n\t"
    "v_mul_f32 v228, s50, v228\n\t"
    "v_mul_f32 v229, s50, v229\n\t"
    "v_mul_f32 v230, s50, v230\n\t"
    "v_mul_f32 v231, s50, v231\n\t"
    "v_cvt_pk_bf16_f32 v228, v228, v229\n\t"
    "v_cvt_pk_bf16_f32 v229, v230, v231\n\t"
    "buffer_store_dwordx2 v[228:229], v21, s[20:23], s18 offen sc1\n\t"
    "s_branch .Lfmm_bk0\n\t"
    ".Lfmm_sd1:\n\t"
    "s_waitcnt vmcnt(0)\n\t"
    "s_mov_b64 exec, 1\n\t"
    "buffer_store_dwordx2 v[12:13], off, s[20:23], s94 sc1\n\t"
    "s_mov_b64 exec, -1\n\t"
    "s_branch .Lfmm_bk1\n\t"
    ".Lfmm_sd2:\n\t"
    "s_lshl_b32 s55, s44, 10\n\t"
    "s_add_u32 s55, s55, s19\n\t"
    "buffer_load_dwordx4 v[248:251], v4, s[20:23], s55 offen sc1\n\t"
    "s_branch .Lfmm_bk2\n\t"
    ".Lfmm_sd3:\n\t"
    "s_waitcnt vmcnt(0)\n\t"
    "v_cmp_eq_u64 s[62:63], v[12:13], v[248:249]\n\t"
    "v_cmp_eq_u64 s[64:65], v[12:13], v[250:251]\n\t"
    "s_nop 0\n\t"
    "s_and_b64 s[62:63], s[62:63], s[64:65]\n\t"
    "s_cmp_eq_u64 s[62:63], -1\n\t"
    "s_cbranch_scc1 21f\n\t"
    "s_mov_b32 s56, 0\n\t"
    "20:\n\t"
    "s_sleep 4\n\t"
    "buffer_load_dwordx4 v[248:251], v4, s[20:23], s55 offen sc1\n\t"
    "s_add_u32 s56, s56, 1\n\t"
    "s_waitcnt vmcnt(0)\n\t"
    "s_cmp_gt_u32 s56, 0x40000\n\t"
    "s_cbranch_scc1 21f\n\t"
    "v_cmp_eq_u64 s[62:63], v[12:13], v[248:249]\n\t"
    "v_cmp_eq_u64 s[64:65], v[12:13], v[250:251]\n\t"
    "s_nop 0\n\t"
    "s_and_b64 s[62:63], s[62:63], s[64:65]\n\t"
    "s_cmp_eq_u64 s[62:63], -1\n\t"
    "s_cbranch_scc0 20b\n\t"
    "21:\n\t"
    "s_branch .Lfmm_bk3\n\t"
    ".Lfmm_sd4:\n\t"
    "s_cmp_lg_u32 s2, 0\n\t"
    "s_cbranch_scc1 22f\n\t"
    "s_mov_b64 exec, 1\n\t"
    "buffer_store_dwordx2 v[12:13], off, s[20:23], s19 offset:2048 sc1\n\t"
    "s_mov_b64 exec, -1\n\t"
    "22:\n\t"
    "s_branch .Lfmm_bk4\n\t"
    ".Lfmm_lnb:\n\t"
    "s_add_u32 m0, s69, 147456\n\t"
    "s_nop 0\n\t"
    "buffer_load_dwordx4 v60, s[12:15], 0 offen lds\n\t"
    "s_add_u32 m0, s69, 148480\n\t"
    "s_nop 0\n\t"
    "buffer_load_dwordx4 v61, s[12:15], 0 offen lds\n\t"
    "s_add_u32 m0, s69, 155648\n\t"
    "s_nop 0\n\t"
    "buffer_load_dwordx4 v62, s[12:15], 0 offen lds\n\t"
    "s_add_u32 m0, s69, 156672\n\t"
    "s_nop 0\n\t"
    "buffer_load_dwordx4 v63, s[12:15], 0 offen lds\n\t"
    "s_add_u32 s60, s61, 0\n\t"
    "s_and_b32 s60, s60, 511\n\t"
    "s_mul_i32 s60, s60, 3584\n\t"
    "s_add_u32 m0, s68, 0\n\t"
    "s_add_u32 s60, s60, s47\n\t"
    "buffer_load_dwordx4 v236, s[8:11], s60 offen offset:0 lds\n\t"
    "buffer_load_dwordx4 v237, s[8:11], s60 offen offset:1024 lds\n\t"
    "buffer_load_dwordx4 v238, s[8:11], s60 offen offset:2048 lds\n\t"
    "buffer_load_dwordx4 v239, s[8:11], s60 offen offset:3072 lds\n\t"
    "s_setprio 0\n\t"
    "s_waitcnt vmcnt(15)\n\t"
    "v_lshlrev_b32 v132, 16, v100\n\t"
    "v_and_b32 v133, 0xffff0000, v100\n\t"
    "v_lshlrev_b32 v134, 16, v101\n\t"
    "v_and_b32 v135, 0xffff0000, v101\n\t"
    "v_lshlrev_b32 v136, 16, v102\n\t"
    "v_and_b32 v137, 0xffff0000, v102\n\t"
    "v_lshlrev_b32 v138, 16, v103\n\t"
    "v_and_b32 v139, 0xffff0000, v103\n\t"
    "v_pk_add_f32 v[204:205], v[132:133], v[136:137]\n\t"
    "v_pk_mul_f32 v[208:209], v[132:133], v[132:133]\n\t"
    "v_pk_add_f32 v[206:207], v[134:135], v[138:139]\n\t"
    "v_pk_mul_f32 v[210:211], v[134:135], v[134:135]\n\t"
    "v_pk_fma_f32 v[208:209], v[136:137], v[136:137], v[208:209]\n\t"
    "v_pk_fma_f32 v[210:211], v[138:139], v[138:139], v[210:211]\n\t"
    "s_waitcnt vmcnt(14)\n\t"
    "v_lshlrev_b32 v140, 16, v104\n\t"
    "v_and_b32 v141, 0xffff0000, v104\n\t"
    "v_lshlrev_b32 v142, 16, v105\n\t"
    "v_and_b32 v143, 0xffff0000, v105\n\t"
    "v_lshlrev_b32 v144, 16, v106\n\t"
    "v_and_b32 v145, 0xffff0000, v106\n\t"
    "v_lshlrev_b32 v146, 16, v107\n\t"
    "v_and_b32 v147, 0xffff0000, v107\n\t"
    "v_pk_add_f32 v[204:205], v[204:205], v[140:141]\n\t"
    "v_pk_fma_f32 v[208:209], v[140:141], v[140:141], v[208:209]\n\t"
    "v_pk_add_f32 v[206:207], v[206:207], v[142:143]\n\t"
    "v_pk_fma_f32 v[210:211], v[142:143], v[142:143], v[210:211]\n\t"
    "v_pk_add_f32 v[204:205], v[204:205], v[144:145]\n\t"
    "v_pk_fma_f32 v[208:209], v[144:145], v[144:145], v[208:209]\n\t"
    "v_pk_add_f32 v[206:207], v[206:207], v[146:147]\n\t"
    "v_pk_fma_f32 v[210:211], v[146:147], v[146:147], v[210:211]\n\t"
    "s_waitcnt vmcnt(13)\n\t"
    "v_lshlrev_b32 v148, 16, v108\n\t"
    "v_and_b32 v149, 0xffff0000, v108\n\t"
    "v_lshlrev_b32 v150, 16, v109\n\t"
    "v_and_b32 v151, 0xffff0000, v109\n\t"
    "v_lshlrev_b32 v152, 16, v110\n\t"
    "v_and_b32 v153, 0xffff0000, v110\n\t"
    "v_lshlrev_b32 v154, 16, v111\n\t"
    "v_and_b32 v155, 0xffff0000, v111\n\t"
    "v_pk_add_f32 v[204:205], v[204:205], v[148:149]\n\t"
    "v_pk_fma_f32 v[208:209], v[148:149], v[148:149], v[208:209]\n\t"
    "v_pk_add_f32 v[206:207], v[206:207], v[150:151]\n\t"
    "v_pk_fma_f32 v[210:211], v[150:151], v[150:151], v[210:211]\n\t"
    "v_pk_add_f32 v[204:205], v[204:205], v[152:153]\n\t"
    "v_pk_fma_f32 v[208:209], v[152:153], v[152:153], v[208:209]\n\t"
    "v_pk_add_f32 v[206:207], v[206:207], v[154:155]\n\t"
    "v_pk_fma_f32 v[210:211], v[154:155], v[154:155], v[210:211]\n\t"
    "s_waitcnt vmcnt(12)\n\t"
    "v_lshlrev_b32 v156, 16, v112\n\t"
    "v_and_b32 v157, 0xffff0000, v112\n\t"
    "v_lshlrev_b32 v158, 16, v113\n\t"
    "v_and_b32 v159, 0xffff0000, v113\n\t"
    "v_lshlrev_b32 v160, 16, v114\n\t"
    "v_and_b32 v161, 0xffff0000, v114\n\t"
    "v_lshlrev_b32 v162, 16, v115\n\t"
    "v_and_b32 v163, 0xffff0000, v115\n\t"
    "v_pk_add_f32 v[204:205], v[204:205], v[156:157]\n\t"
    "v_pk_fma_f32 v[208:209], v[156:157], v[156:157], v[208:209]\n\t"
    "v_pk_add_f32 v[206:207], v[206:207], v[158:159]\n\t"
    "v_pk_fma_f32 v[210:211], v[158:159], v[158:159], v[210:211]\n\t"
    "v_pk_add_f32 v[204:205], v[204:205], v[160:161]\n\t"
    "v_pk_fma_f32 v[208:209], v[160:161], v[160:161], v[208:209]\n\t"
    "v_pk_add_f32 v[206:207], v[206:207], v[162:163]\n\t"
    "v_pk_fma_f32 v[210:211], v[162:163], v[162:163], v[210:211]\n\t"
    "s_waitcnt vmcnt(11)\n\t"
    "v_lshlrev_b32 v164, 16, v116\n\t"
    "v_and_b32 v165, 0xffff0000, v116\n\t"
    "v_lshlrev_b32 v166, 16, v117\n\t"
    "v_and_b32 v167, 0xffff0000, v117\n\t"
    "v_lshlrev_b32 v168, 16, v118\n\t"
    "v_and_b32 v169, 0xffff0000, v118\n\t"
    "v_lshlrev_b32 v170, 16, v119\n\t"
    "v_and_b32 v171, 0xffff0000, v119\n\t"
    "v_pk_add_f32 v[204:205], v[204:205], v[164:165]\n\t"
    "v_pk_fma_f32 v[208:209], v[164:165], v[164:165], v[208:209]\n\t"
    "v_pk_add_f32 v[206:207], v[206:207], v[166:167]\n\t"
    "v_pk_fma_f32 v[210:211], v[166:167], v[166:167], v[210:211]\n\t"
    "v_pk_add_f32 v[204:205], v[204:205], v[168:169]\n\t"
    "v_pk_fma_f32 v[208:209], v[168:169], v[168:169], v[208:209]\n\t"
    "v_pk_add_f32 v[206:207], v[206:207], v[170:171]\n\t"
    "v_pk_fma_f32 v[210:211], v[170:171], v[170:171], v[210:211]\n\t"
    "s_waitcnt vmcnt(10)\n\t"
    "v_lshlrev_b32 v172, 16, v120\n\t"
    "v_and_b32 v173, 0xffff0000, v120\n\t"
    "v_lshlrev_b32 v174, 16, v121\n\t"
    "v_and_b32 v175, 0xffff0000, v121\n\t"
    "v_lshlrev_b32 v176, 16, v122\n\t"
    "v_and_b32 v177, 0xffff0000, v122\n\t"
    "v_lshlrev_b32 v178, 16, v123\n\t"
    "v_and_b32 v179, 0xffff0000, v123\n\t"
    "v_pk_add_f32 v[204:205], v[204:205], v[172:173]\n\t"
    "v_pk_fma_f32 v[208:209], v[172:173], v[172:173], v[208:209]\n\t"
    "v_pk_add_f32 v[206:207], v[206:207], v[174:175]\n\t"
    "v_pk_fma_f32 v[210:211], v[174:175], v[174:175], v[210:211]\n\t"
    "v_pk_add_f32 v[204:205], v[204:205], v[176:177]\n\t"
    "v_pk_fma_f32 v[208:209], v[176:177], v[176:177], v[208:209]\n\t"
    ".p2align 12\n\t"
    ".Lfmm_page1:\n\t"
    "s_cbranch_execz .Lfmm_ret\n\t"
    "v_pk_add_f32 v[206:207], v[206:207], v[178:179]\n\t"
    "v_pk_fma_f32 v[210:211], v[178:179], v[178:179], v[210:211]\n\t"
    "s_waitcnt vmcnt(9)\n\t"
    "v_lshlrev_b32 v180, 16, v124\n\t"
    "v_and_b32 v181, 0xffff0000, v124\n\t"
    "v_lshlrev_b32 v182, 16, v125\n\t"
    "v_and_b32 v183, 0xffff0000, v125\n\t"
    "v_lshlrev_b32 v184, 16, v126\n\t"
    "v_and_b32 v185, 0xffff0000, v126\n\t"
    "v_lshlrev_b32 v186, 16, v127\n\t"
    "v_and_b32 v187, 0xffff0000, v127\n\t"
    "v_pk_add_f32 v[204:205], v[204:205], v[180:181]\n\t"
    "v_pk_fma_f32 v[208:209], v[180:181], v[180:181], v[208:209]\n\t"
    "v_pk_add_f32 v[206:207], v[206:207], v[182:183]\n\t"
    "v_pk_fma_f32 v[210:211], v[182:183], v[182:183], v[210:211]\n\t"
    "v_pk_add_f32 v[204:205], v[204:205], v[184:185]\n\t"
    "v_pk_fma_f32 v[208:209], v[184:185], v[184:185], v[208:209]\n\t"
    "v_pk_add_f32 v[206:207], v[206:207], v[186:187]\n\t"
    "v_pk_fma_f32 v[210:211], v[186:187], v[186:187], v[210:211]\n\t"
    "s_waitcnt vmcnt(8)\n\t"
    "v_lshlrev_b32 v188, 16, v128\n\t"
    "v_and_b32 v189, 0xffff0000, v128\n\t"
    "v_lshlrev_b32 v190, 16, v129\n\t"
    "v_and_b32 v191, 0xffff0000, v129\n\t"
    "v_lshlrev_b32 v192, 16, v130\n\t"
    "v_and_b32 v193, 0xffff0000, v130\n\t"
    "v_lshlrev_b32 v194, 16, v131\n\t"
    "v_and_b32 v195, 0xffff0000, v131\n\t"
    "v_pk_add_f32 v[204:205], v[204:205], v[188:189]\n\t"
    "v_pk_fma_f32 v[208:209], v[188:189], v[188:189], v[208:209]\n\t"
    "v_pk_add_f32 v[206:207], v[206:207], v[190:191]\n\t"
    "v_pk_fma_f32 v[210:211], v[190:191], v[190:191], v[210:211]\n\t"
    "v_pk_add_f32 v[204:205], v[204:205], v[192:193]\n\t"
    "v_pk_fma_f32 v[208:209], v[192:193], v[192:193], v[208:209]\n\t"
    "v_pk_add_f32 v[206:207], v[206:207], v[194:195]\n\t"
    "v_pk_fma_f32 v[210:211], v[194:195], v[194:195], v[210:211]\n\t"
    "v_pk_add_f32 v[204:205], v[204:205], v[206:207]\n\t"
    "v_pk_add_f32 v[208:209], v[208:209], v[210:211]\n\t"
    "v_add_f32 v204, v204, v205\n\t"
    "v_add_f32 v208, v208, v209\n\t"
    "s_nop 0\n\t"
    "v_add_f32_dpp v204, v204, v204 quad_perm:[1,0,3,2] row_mask:0xf bank_mask:0xf bound_ctrl:1\n\t"
    "v_add_f32_dpp v208, v208, v208 quad_perm:[1,0,3,2] row_mask:0xf bank_mask:0xf bound_ctrl:1\n\t"
    "s_nop 0\n\t"
    "v_add_f32_dpp v204, v204, v204 quad_perm:[2,3,0,1] row_mask:0xf bank_mask:0xf bound_ctrl:1\n\t"
    "v_add_f32_dpp v208, v208, v208 quad_perm:[2,3,0,1] row_mask:0xf bank_mask:0xf bound_ctrl:1\n\t"
    "s_nop 0\n\t"
    "v_add_f32_dpp v204, v204, v204 row_half_mirror row_mask:0xf bank_mask:0xf bound_ctrl:1\n\t"
    "v_add_f32_dpp v208, v208, v208 row_half_mirror row_mask:0xf bank_mask:0xf bound_ctrl:1\n\t"
    "v_mul_f32 v204, 0x3b000000, v204\n\t"
    "v_mul_f32 v208, 0x3b000000, v208\n\t"
    "v_fma_f32 v208, -v204, v204, v208\n\t"
    "v_max_f32 v208, 0, v208\n\t"
    "v_add_f32 v208, 0x3727c5ac, v208\n\t"
    "v_rsq_f32 v208, v208\n\t"
    "s_nop 0\n\t"
    "v_mul_f32 v208, 0x3f38aa3b, v208\n\t"
    "v_mul_f32 v210, -v204, v208\n\t"
    "v_pk_fma_f32 v[132:133], v[132:133], v[208:209], v[210:211] op_sel_hi:[1,0,0]\n\t"
    "v_pk_fma_f32 v[134:135], v[134:135], v[208:209], v[210:211] op_sel_hi:[1,0,0]\n\t"
    "v_pk_fma_f32 v[136:137], v[136:137], v[208:209], v[210:211] op_sel_hi:[1,0,0]\n\t"
    "v_pk_fma_f32 v[138:139], v[138:139], v[208:209], v[210:211] op_sel_hi:[1,0,0]\n\t"
    "v_cvt_pk_bf16_f32 v100, v132, v133\n\t"
    "v_cvt_pk_bf16_f32 v101, v134, v135\n\t"
    "v_cvt_pk_bf16_f32 v102, v136, v137\n\t"
    "v_cvt_pk_bf16_f32 v103, v138, v139\n\t"
    "ds_write_b128 v196, v[100:103] offset:32768\n\t"
    "v_pk_fma_f32 v[140:141], v[140:141], v[208:209], v[210:211] op_sel_hi:[1,0,0]\n\t"
    "v_pk_fma_f32 v[142:143], v[142:143], v[208:209], v[210:211] op_sel_hi:[1,0,0]\n\t"
    "v_pk_fma_f32 v[144:145], v[144:145], v[208:209], v[210:211] op_sel_hi:[1,0,0]\n\t"
    "v_pk_fma_f32 v[146:147], v[146:147], v[208:209], v[210:211] op_sel_hi:[1,0,0]\n\t"
    "v_cvt_pk_bf16_f32 v104, v140, v141\n\t"
    "v_cvt_pk_bf16_f32 v105, v142, v143\n\t"
    "v_cvt_pk_bf16_f32 v106, v144, v145\n\t"
    "v_cvt_pk_bf16_f32 v107, v146, v147\n\t"
    "ds_write_b128 v197, v[104:107] offset:32768\n\t"
    "v_pk_fma_f32 v[148:149], v[148:149], v[208:209], v[210:211] op_sel_hi:[1,0,0]\n\t"
    "v_pk_fma_f32 v[150:151], v[150:151], v[208:209], v[210:211] op_sel_hi:[1,0,0]\n\t"
    "v_pk_fma_f32 v[152:153], v[152:153], v[208:209], v[210:211] op_sel_hi:[1,0,0]\n\t"
    "v_pk_fma_f32 v[154:155], v[154:155], v[208:209], v[210:211] op_sel_hi:[1,0,0]\n\t"
    "v_cvt_pk_bf16_f32 v108, v148, v149\n\t"
    "v_cvt_pk_bf16_f32 v109, v150, v151\n\t"
    "v_cvt_pk_bf16_f32 v110, v152, v153\n\t"
    "v_cvt_pk_bf16_f32 v111, v154, v155\n\t"
    "ds_write_b128 v198, v[108:111] offset:32768\n\t"
    "v_pk_fma_f32 v[156:157], v[156:157], v[208:209], v[210:211] op_sel_hi:[1,0,0]\n\t"
    "v_pk_fma_f32 v[158:159], v[158:159], v[208:209], v[210:211] op_sel_hi:[1,0,0]\n\t"
    "v_pk_fma_f32 v[160:161], v[160:161], v[208:209], v[210:211] op_sel_hi:[1,0,0]\n\t"
    "v_pk_fma_f32 v[162:163], v[162:163], v[208:209], v[210:211] op_sel_hi:[1,0,0]\n\t"
    "v_cvt_pk_bf16_f32 v112, v156, v157\n\t"
    "v_cvt_pk_bf16_f32 v113, v158, v159\n\t"
    "v_cvt_pk_bf16_f32 v114, v160, v161\n\t"
    "v_cvt_pk_bf16_f32 v115, v162, v163\n\t"
    "ds_write_b128 v199, v[112:115] offset:32768\n\t"
    "v_pk_fma_f32 v[164:165], v[164:165], v[208:209], v[210:211] op_sel_hi:[1,0,0]\n\t"
    "v_pk_fma_f32 v[166:167], v[166:167], v[208:209], v[210:211] op_sel_hi:[1,0,0]\n\t"
    "v_pk_fma_f32 v[168:169], v[168:169], v[208:209], v[210:211] op_sel_hi:[1,0,0]\n\t"
    "v_pk_fma_f32 v[170:171], v[170:171], v[208:209], v[210:211] op_sel_hi:[1,0,0]\n\t"
    "v_cvt_pk_bf16_f32 v116, v164, v165\n\t"
    "v_cvt_pk_bf16_f32 v117, v166, v167\n\t"
    "v_cvt_pk_bf16_f32 v118, v168, v169\n\t"
    "v_cvt_pk_bf16_f32 v119, v170, v171\n\t"
    "ds_write_b128 v200, v[116:119] offset:32768\n\t"
    "v_pk_fma_f32 v[172:173], v[172:173], v[208:209], v[210:211] op_sel_hi:[1,0,0]\n\t"
    "v_pk_fma_f32 v[174:175], v[174:175], v[208:209], v[210:211] op_sel_hi:[1,0,0]\n\t"
    "v_pk_fma_f32 v[176:177], v[176:177], v[208:209], v[210:211] op_sel_hi:[1,0,0]\n\t"
    "v_pk_fma_f32 v[178:179], v[178:179], v[208:209], v[210:211] op_sel_hi:[1,0,0]\n\t"
    "v_cvt_pk_bf16_f32 v120, v172, v173\n\t"
    "v_cvt_pk_bf16_f32 v121, v174, v175\n\t"
    "v_cvt_pk_bf16_f32 v122, v176, v177\n\t"
    "v_cvt_pk_bf16_f32 v123, v178, v179\n\t"
    "ds_write_b128 v201, v[120:123] offset:32768\n\t"
    "v_pk_fma_f32 v[180:181], v[180:181], v[208:209], v[210:211] op_sel_hi:[1,0,0]\n\t"
    "v_pk_fma_f32 v[182:183], v[182:183], v[208:209], v[210:211] op_sel_hi:[1,0,0]\n\t"
    "v_pk_fma_f32 v[184:185], v[184:185], v[208:209], v[210:211] op_sel_hi:[1,0,0]\n\t"
    "v_pk_fma_f32 v[186:187], v[186:187], v[208:209], v[210:211] op_sel_hi:[1,0,0]\n\t"
    "v_cvt_pk_bf16_f32 v124, v180, v181\n\t"
    "v_cvt_pk_bf16_f32 v125, v182, v183\n\t"
    "v_cvt_pk_bf16_f32 v126, v184, v185\n\t"
    "v_cvt_pk_bf16_f32 v127, v186, v187\n\t"
    "ds_write_b128 v202, v[124:127] offset:32768\n\t"
    "v_pk_fma_f32 v[188:189], v[188:189], v[208:209], v[210:211] op_sel_hi:[1,0,0]\n\t"
    "v_pk_fma_f32 v[190:191], v[190:191], v[208:209], v[210:211] op_sel_hi:[1,0,0]\n\t"
    "v_pk_fma_f32 v[192:193], v[192:193], v[208:209], v[210:211] op_sel_hi:[1,0,0]\n\t"
    "v_pk_fma_f32 v[194:195], v[194:195], v[208:209], v[210:211] op_sel_hi:[1,0,0]\n\t"
    "v_cvt_pk_bf16_f32 v128, v188, v189\n\t"
    "v_cvt_pk_bf16_f32 v129, v190, v191\n\t"
    "v_cvt_pk_bf16_f32 v130, v192, v193\n\t"
    "v_cvt_pk_bf16_f32 v131, v194, v195\n\t"
    "ds_write_b128 v203, v[128:131] offset:32768\n\t"
    "s_waitcnt vmcnt(0)\n\t"
    "s_add_u32 s15, s61, 528\n\t"
    "s_add_u32 s61, s61, 496\n\t"
    "s_movk_i32 s69, 8192\n\t"
    "s_setprio 0\n\t"
    ".Lfmm_lnend:\n\t"
    "v_lshl_or_b32 v28, v28, 16, v28\n\t"
    "v_lshl_or_b32 v30, v30, 16, v30\n\t"
    "v_mov_b32 v29, v28\n\t"
    "v_mov_b32 v31, v30\n\t"
    "s_cmp_eq_u32 s49, 3\n\t"
    "s_nop 0\n\t"
    "v_mfma_f32_32x32x8_bf16 v[64:79], v[28:29], v[2:3], 0\n\t"
    "v_mfma_f32_32x32x8_bf16 v[80:95], v[30:31], v[2:3], 0\n\t"
    "s_waitcnt lgkmcnt(0)\n\t"
    "s_barrier\n\t"
    "s_cbranch_scc1 .Lfmm_roleb\n\t"
    "ds_read_b64_tr_b16 v[32:33], v96 offset:0\n\t"
    "ds_read_b64_tr_b16 v[34:35], v96 offset:1024\n\t"
    "ds_read_b128 v[100:103], v16 offset:0\n\t"
    "ds_read_b64_tr_b16 v[36:37], v96 offset:4096\n\t"
    "ds_read_b64_tr_b16 v[38:39], v96 offset:5120\n\t"
    "ds_read_b128 v[104:107], v17 offset:0\n\t"
    "ds_read_b64_tr_b16 v[40:41], v97 offset:0\n\t"
    "ds_read_b64_tr_b16 v[42:43], v97 offset:1024\n\t"
    "ds_read_b128 v[108:111], v18 offset:0\n\t"
    "ds_read_b64_tr_b16 v[44:45], v97 offset:4096\n\t"
    "ds_read_b64_tr_b16 v[46:47], v97 offset:5120\n\t"
    "ds_read_b128 v[112:115], v19 offset:0\n\t"
    "s_cmp_lt_u32 s44, 2\n\t"
    "s_cselect_b64 vcc, -1, 0\n\t"
    "s_waitcnt vmcnt(4)\n\t"
    "s_barrier\n\t"
    "s_waitcnt lgkmcnt(6)\n\t"
    "v_mfma_f32_32x32x16_bf16 v[64:79], v[32:35], v[100:103], v[64:79]\n\t"
    "ds_read_b64_tr_b16 v[48:49], v96 offset:16384\n\t"
    "ds_read_b64_tr_b16 v[50:51], v96 offset:17408\n\t"
    "ds_read_b128 v[116:119], v16 offset:8192\n\t"
    "v_add_u32 v98, 98304, v96\n\t"
    "v_add_u32 v99, 98304, v97\n\t"
    "s_setreg_imm32_b32 hwreg(HW_REG_MODE, 4, 6), 0x1c\n\t"
    "s_lshl3_add_u32 s99, s94, s45\n\t"
    "s_mov_b32 s19, 131072\n\t"
    "v_mfma_f32_32x32x16_bf16 v[64:79], v[36:39], v[104:107], v[64:79]\n\t"
    "ds_read_b64_tr_b16 v[52:53], v96 offset:20480\n\t"
    "ds_read_b64_tr_b16 v[54:55], v96 offset:21504\n\t"
    "ds_read_b128 v[120:123], v17 offset:8192\n\t"
    "s_lshl3_add_u32 s94, s99, s19\n\t"
    "s_lshl3_add_u32 s3, s99, s44\n\t"
    "s_mul_i32 s64, s99, 28\n\t"
    "s_brev_b32 s62, s49\n\t"
    "s_waitcnt lgkmcnt(6)\n\t"
    "v_mfma_f32_32x32x16_bf16 v[64:79], v[40:43], v[108:111], v[64:79]\n\t"
    "ds_read_b64_tr_b16 v[56:57], v97 offset:16384\n\t"
    "ds_read_b64_tr_b16 v[58:59], v97 offset:17408\n\t"
    "ds_read_b128 v[124:127], v18 offset:8192\n\t"
    "ds_read_b64_tr_b16 v[60:61], v97 offset:20480\n\t"
    "ds_read_b64_tr_b16 v[62:63], v97 offset:21504\n\t"
    "ds_read_b128 v[128:131], v19 offset:8192\n\t"
    "s_lshr_b32 s62, s62, 30\n\t"
    "s_lshl1_add_u32 s63, s99, s98\n\t"
    "s_lshl2_add_u32 s63, s63, s62\n\t"
    "s_lshl_b32 s63, s63, 10\n\t"
    "v_mfma_f32_32x32x16_bf16 v[64:79], v[44:47], v[112:115], v[64:79]\n\t"
    "s_waitcnt vmcnt(0)\n\t"
    "s_barrier\n\t"
    "s_waitcnt lgkmcnt(6)\n\t"
    "v_mfma_f32_32x32x16_bf16 v[64:79], v[48:51], v[116:119], v[64:79]\n\t"
    "ds_read_b64_tr_b16 v[32:33], v98 offset:0\n\t"
    "ds_read_b64_tr_b16 v[34:35], v98 offset:1024\n\t"
    "ds_read_b128 v[132:135], v16 offset:16384\n\t"
    "s_add_u32 s99, s63, 262144\n\t"
    "s_mul_i32 s62, s66, 0x5851f42d\n\t"
    "s_mul_hi_u32 s63, s66, 0x4c957f2d\n\t"
    "s_mul_i32 s67, s67, 0x4c957f2d\n\t"
    "s_add_u32 s62, s62, s63\n\t"
    "v_mfma_f32_32x32x16_bf16 v[64:79], v[52:55], v[120:123], v[64:79]\n\t"
    "ds_read_b64_tr_b16 v[36:37], v98 offset:4096\n\t"
    "ds_read_b64_tr_b16 v[38:39], v98 offset:5120\n\t"
    "ds_read_b128 v[136:139], v17 offset:16384\n\t"
    "s_mul_i32 s66, s66, 0x4c957f2d\n\t"
    "s_add_u32 s67, s67, s62\n\t"
    "s_add_u32 s66, s66, 0xf767814f\n\t"
    "s_addc_u32 s67, s67, 0x14057b7e\n\t"
    "v_mov_b32 v12, s66\n\t"
    "s_waitcnt lgkmcnt(6)\n\t"
    "v_mfma_f32_32x32x16_bf16 v[64:79], v[56:59], v[124:127], v[64:79]\n\t"
    "ds_read_b64_tr_b16 v[40:41], v99 offset:0\n\t"
    "ds_read_b64_tr_b16 v[42:43], v99 offset:1024\n\t"
    "ds_read_b128 v[140:143], v18 offset:16384\n\t"
    "ds_read_b64_tr_b16 v[44:45], v99 offset:4096\n\t"
    "ds_read_b64_tr_b16 v[46:47], v99 offset:5120\n\t"
    "ds_read_b128 v[144:147], v19 offset:16384\n\t"
    "v_mov_b32 v13, s67\n\t"
    "s_lshl3_add_u32 s3, s3, 163840\n\t"
    "s_add_u32 s64, s64, s98\n\t"
    "s_add_u32 s64, s64, 4096\n\t"
    "v_mfma_f32_32x32x16_bf16 v[64:79], v[60:63], v[128:131], v[64:79]\n\t"
    "s_barrier\n\t"
    "s_waitcnt lgkmcnt(6)\n\t"
    "v_mfma_f32_32x32x16_bf16 v[64:79], v[32:35], v[132:135], v[64:79]\n\t"
    "ds_read_b64_tr_b16 v[48:49], v98 offset:16384\n\t"
    "ds_read_b64_tr_b16 v[50:51], v98 offset:17408\n\t"
    "ds_read_b128 v[148:151], v16 offset:24576\n\t"
    "s_lshl2_add_u32 s16, s16, s64\n\t"
    "s_lshl2_add_u32 s17, s17, s64\n\t"
    "s_lshl_b32 s16, s16, 10\n\t"
    "s_lshl_b32 s17, s17, 10\n\t"
    "s_lshl_b32 s62, s96, 8\n\t"
    "ds_read_b128 v[228:231], v25 offset:6144\n\t"
    "ds_read_b128 v[232:235], v25 offset:14336\n\t"
    "v_mfma_f32_32x32x16_bf16 v[64:79], v[36:39], v[136:139], v[64:79]\n\t"
    "ds_read_b64_tr_b16 v[52:53], v98 offset:20480\n\t"
    "ds_read_b64_tr_b16 v[54:55], v98 offset:21504\n\t"
    "ds_read_b128 v[152:155], v17 offset:24576\n\t"
    "s_sub_u32 s96, 0, s62\n\t"
    "v_bfe_u32 v1, v0, 5, 1\n\t"
    "v_lshl_or_b32 v10, v1, 10, v26\n\t"
    "s_and_b32 s62, s44, 2\n\t"
    "s_waitcnt lgkmcnt(8)\n\t"
    "v_mfma_f32_32x32x16_bf16 v[64:79], v[40:43], v[140:143], v[64:79]\n\t"
    "ds_read_b64_tr_b16 v[56:57], v99 offset:16384\n\t"
    "ds_read_b64_tr_b16 v[58:59], v99 offset:17408\n\t"
    "ds_read_b128 v[156:159], v18 offset:24576\n\t"
    "ds_read_b64_tr_b16 v[60:61], v99 offset:20480\n\t"
    "ds_read_b64_tr_b16 v[62:63], v99 offset:21504\n\t"
    "ds_read_b128 v[160:163], v19 offset:24576\n\t"
    "s_and_b32 s64, s44, 1\n\t"
    "s_lshl2_add_u32 s64, s62, s64\n\t"
    "s_lshl_b32 s65, s62, 4\n\t"
    "v_xor_b32 v254, s65, v10\n\t"
    "s_cmp_eq_u32 s44, 0\n\t"
    "s_cbranch_scc1 .Lfmm_sd0\n\t"
    ".Lfmm_bk0:\n\t"
    "v_mfma_f32_32x32x16_bf16 v[64:79], v[44:47], v[144:147], v[64:79]\n\t"
    "s_barrier\n\t"
    "s_waitcnt lgkmcnt(6)\n\t"
    "v_mfma_f32_32x32x16_bf16 v[64:79], v[48:51], v[148:151], v[64:79]\n\t"
    "ds_read_b64_tr_b16 v[32:33], v98 offset:32768\n\t"
    "ds_read_b64_tr_b16 v[34:35], v98 offset:33792\n\t"
    "ds_read_b128 v[164:167], v16 offset:32768\n\t"
    "v_lshl_add_u32 v254, s64, 9, v254\n\t"
    "v_add_u32 v255, 98304, v254\n\t"
    "s_add_u32 s62, s46, 131072\n\t"
    "v_add_u32 v10, s62, v10\n\t"
    "v_and_b32 v5, 0xf0, v4\n\t"
    "v_mfma_f32_32x32x16_bf16 v[64:79], v[52:55], v[152:155], v[64:79]\n\t"
    "ds_read_b64_tr_b16 v[36:37], v98 offset:36864\n\t"
    "ds_read_b64_tr_b16 v[38:39], v98 offset:37888\n\t"
    "ds_read_b128 v[168:171], v17 offset:32768\n\t"
    "v_lshl_or_b32 v5, v1, 8, v5\n\t"
    "s_and_b32 s62, s44, 2\n\t"
    "s_lshl_b32 s62, s62, 10\n\t"
    "s_add_u32 s62, s62, 32768\n\t"
    "s_waitcnt lgkmcnt(6)\n\t"
    "v_mfma_f32_32x32x16_bf16 v[64:79], v[56:59], v[156:159], v[64:79]\n\t"
    "ds_read_b64_tr_b16 v[40:41], v99 offset:32768\n\t"
    "ds_read_b64_tr_b16 v[42:43], v99 offset:33792\n\t"
    "ds_read_b128 v[172:175], v18 offset:32768\n\t"
    "ds_read_b64_tr_b16 v[44:45], v99 offset:36864\n\t"
    "ds_read_b64_tr_b16 v[46:47], v99 offset:37888\n\t"
    "ds_read_b128 v[176:179], v19 offset:32768\n\t"
    "v_add_u32 v14, s62, v4\n\t"
    "s_add_u32 s62, s62, 0x400\n\t"
    "v_add_u32 v5, s62, v5\n\t"
    "v_and_b32 v1, 31, v0\n\t"
    "v_mfma_f32_32x32x16_bf16 v[64:79], v[60:63], v[160:163], v[64:79]\n\t"
    "s_barrier\n\t"
    "s_waitcnt lgkmcnt(6)\n\t"
    "v_mfma_f32_32x32x16_bf16 v[64:79], v[32:35], v[164:167], v[64:79]\n\t"
    "ds_read_b64_tr_b16 v[48:49], v96 offset:0\n\t"
    "ds_read_b64_tr_b16 v[50:51], v96 offset:1024\n\t"
    "ds_read_b128 v[180:183], v16 offset:40960\n\t"
    "v_cmp_eq_u32 s[64:65], 16, v1\n\t"
    "v_mov_b32 v1, 0x3f003f00\n\t"
    "v_add_u32 v29, s46, v4\n\t"
    "v_cndmask_b32 v11, 0, v1, s[64:65]\n\t"
    "v_xor_b32 v28, 0x1000, v29\n\t"
    "v_mfma_f32_32x32x16_bf16 v[64:79], v[36:39], v[168:171], v[64:79]\n\t"
    "ds_read_b64_tr_b16 v[52:53], v96 offset:4096\n\t"
    "ds_read_b64_tr_b16 v[54:55], v96 offset:5120\n\t"
    "ds_read_b128 v[184:187], v17 offset:40960\n\t"
    "s_and_b32 s68, s2, 7\n\t"
    "s_lshr_b32 s62, s2, 6\n\t"
    "s_lshl2_add_u32 s58, s68, s62\n\t"
    "s_bfe_u32 s59, s2, 0x30003\n\t"
    "s_waitcnt lgkmcnt(6)\n\t"
    "v_mfma_f32_32x32x16_bf16 v[64:79], v[40:43], v[172:175], v[64:79]\n\t"
    "ds_read_b64_tr_b16 v[56:57], v97 offset:0\n\t"
    "ds_read_b64_tr_b16 v[58:59], v97 offset:1024\n\t"
    "ds_read_b128 v[188:191], v18 offset:40960\n\t"
    "ds_read_b64_tr_b16 v[60:61], v97 offset:4096\n\t"
    "ds_read_b64_tr_b16 v[62:63], v97 offset:5120\n\t"
    "ds_read_b128 v[192:195], v19 offset:40960\n\t"
    "s_lshl3_add_u32 s63, s58, s44\n\t"
    "s_mul_i32 s62, s44, 229376\n\t"
    "s_add_u32 s36, s36, s62\n\t"
    "s_addc_u32 s37, s37, 0\n\t"
    "s_lshl_b32 s62, s44, 16\n\t"
    "v_mfma_f32_32x32x16_bf16 v[64:79], v[44:47], v[176:179], v[64:79]\n\t"
    "s_barrier\n\t"
    "s_waitcnt lgkmcnt(6)\n\t"
    "v_mfma_f32_32x32x16_bf16 v[64:79], v[48:51], v[180:183], v[64:79]\n\t"
    "ds_read_b64_tr_b16 v[32:33], v96 offset:16384\n\t"
    "ds_read_b64_tr_b16 v[34:35], v96 offset:17408\n\t"
    "ds_read_b128 v[196:199], v16 offset:49152\n\t"
    "s_add_u32 s32, s32, s62\n\t"
    "s_addc_u32 s33, s33, 0\n\t"
    "s_mul_i32 s62, s63, 28672\n\t"
    "s_add_u32 s62, s62, 4198400\n\t"
    "s_add_u32 s52, s20, s62\n\t"
    "s_addc_u32 s53, s21, 0\n\t"
    "s_lshl_b32 s57, s63, 13\n\t"
    "v_mfma_f32_32x32x16_bf16 v[64:79], v[52:55], v[184:187], v[64:79]\n\t"
    "ds_read_b64_tr_b16 v[36:37], v96 offset:20480\n\t"
    "ds_read_b64_tr_b16 v[38:39], v96 offset:21504\n\t"
    "ds_read_b128 v[200:203], v17 offset:49152\n\t"
    "s_lshl_b32 s63, s63, 6\n\t"
    "s_add_u32 s57, s57, 262144\n\t"
    "s_add_u32 s63, s63, 163840\n\t"
    "v_and_b32 v1, 7, v0\n\t"
    "s_waitcnt lgkmcnt(6)\n\t"
    "v_mfma_f32_32x32x16_bf16 v[64:79], v[56:59], v[188:191], v[64:79]\n\t"
    "ds_read_b64_tr_b16 v[40:41], v97 offset:16384\n\t"
    "ds_read_b64_tr_b16 v[42:43], v97 offset:17408\n\t"
    "ds_read_b128 v[204:207], v18 offset:49152\n\t"
    "ds_read_b64_tr_b16 v[44:45], v97 offset:20480\n\t"
    "ds_read_b64_tr_b16 v[46:47], v97 offset:21504\n\t"
    "ds_read_b128 v[208:211], v19 offset:49152\n\t"
    "v_lshl_add_u32 v7, v1, 3, s63\n\t"
    "v_lshl_add_u32 v8, s59, 7, v27\n\t"
    "v_add_u32 v9, 7168, v8\n\t"
    "s_cmp_gt_u32 s44, 3\n\t"
    "s_cselect_b32 s51, s57, 0x7ff00000\n\t"
    "s_cmp_eq_u32 s44, 0\n\t"
    "s_cbranch_scc1 .Lfmm_sd1\n\t"
    ".Lfmm_bk1:\n\t"
    "v_mfma_f32_32x32x16_bf16 v[64:79], v[60:63], v[192:195], v[64:79]\n\t"
    "s_barrier\n\t"
    "s_waitcnt lgkmcnt(6)\n\t"
    "v_mfma_f32_32x32x16_bf16 v[64:79], v[32:35], v[196:199], v[64:79]\n\t"
    "ds_read_b64_tr_b16 v[48:49], v98 offset:0\n\t"
    "ds_read_b64_tr_b16 v[50:51], v98 offset:1024\n\t"
    "ds_read_b128 v[212:215], v16 offset:57344\n\t"
    "s_lshr_b32 s62, s2, 3\n\t"
    "s_lshl1_add_u32 s54, s62, s44\n\t"
    "s_add_u32 s1, s54, 16\n\t"
    "s_lshl_b32 s1, s1, 11\n\t"
    "v_mfma_f32_32x32x16_bf16 v[64:79], v[36:39], v[200:203], v[64:79]\n\t"
    "ds_read_b64_tr_b16 v[52:53], v98 offset:4096\n\t"
    "ds_read_b64_tr_b16 v[54:55], v98 offset:5120\n\t"
    "ds_read_b128 v[216:219], v17 offset:57344\n\t"
    "s_lshl3_add_u32 s62, s58, s44\n\t"
    "v_and_b32 v1, 7, v0\n\t"
    "v_lshl_add_u32 v1, s62, 3, v1\n\t"
    "v_lshlrev_b32 v1, 7, v1\n\t"
    "s_waitcnt lgkmcnt(6)\n\t"
    "v_mfma_f32_32x32x16_bf16 v[64:79], v[40:43], v[204:207], v[64:79]\n\t"
    "ds_read_b64_tr_b16 v[56:57], v99 offset:0\n\t"
    "ds_read_b64_tr_b16 v[58:59], v99 offset:1024\n\t"
    "ds_read_b128 v[220:223], v18 offset:57344\n\t"
    "ds_read_b64_tr_b16 v[60:61], v99 offset:4096\n\t"
    "ds_read_b64_tr_b16 v[62:63], v99 offset:5120\n\t"
    "ds_read_b128 v[224:227], v19 offset:57344\n\t"
    "v_lshl_or_b32 v1, s59, 4, v1\n\t"
    "v_bfe_u32 v26, v0, 4, 2\n\t"
    "v_and_or_b32 v26, v0, 8, v26\n\t"
    "v_add_lshl_u32 v1, v1, v26, 3\n\t"
    "v_mfma_f32_32x32x16_bf16 v[64:79], v[44:47], v[208:211], v[64:79]\n\t"
    "s_barrier\n\t"
    "v_add_u32 v96, s96, v96\n\t"
    "v_add_u32 v97, s96, v97\n\t"
    "v_add_u32 v98, s96, v98\n\t"
    "v_add_u32 v99, s96, v99\n\t"
    "s_waitcnt lgkmcnt(6)\n\t"
    "v_mfma_f32_32x32x16_bf16 v[64:79], v[48:51], v[212:215], v[64:79]\n\t"
    "ds_read_b64_tr_b16 v[32:33], v98 offset:16384\n\t"
    "ds_read_b64_tr_b16 v[34:35], v98 offset:17408\n\t"
    "v_mfma_f32_32x32x16_bf16 v[64:79], v[52:55], v[216:219], v[64:79]\n\t"
    "ds_read_b64_tr_b16 v[36:37], v98 offset:20480\n\t"
    "ds_read_b64_tr_b16 v[38:39], v98 offset:21504\n\t"
    "s_waitcnt lgkmcnt(4)\n\t"
    "v_mfma_f32_32x32x16_bf16 v[64:79], v[56:59], v[220:223], v[64:79]\n\t"
    "ds_read_b64_tr_b16 v[40:41], v99 offset:16384\n\t"
    "ds_read_b64_tr_b16 v[42:43], v99 offset:17408\n\t"
    "ds_read_b64_tr_b16 v[44:45], v99 offset:20480\n\t"
    "ds_read_b64_tr_b16 v[46:47], v99 offset:21504\n\t"
    "v_mfma_f32_32x32x16_bf16 v[64:79], v[60:63], v[224:227], v[64:79]\n\t"
    "s_barrier\n\t"
    "s_waitcnt lgkmcnt(4)\n\t"
    "v_mfma_f32_32x32x16_bf16 v[80:95], v[32:35], v[100:103], v[80:95]\n\t"
    "ds_read_b64_tr_b16 v[48:49], v98 offset:32768\n\t"
    "ds_read_b64_tr_b16 v[50:51], v98 offset:33792\n\t"
    "v_mfma_f32_32x32x16_bf16 v[80:95], v[36:39], v[104:107], v[80:95]\n\t"
    "ds_read_b64_tr_b16 v[52:53], v98 offset:36864\n\t"
    "ds_read_b64_tr_b16 v[54:55], v98 offset:37888\n\t"
    "s_nop 3\n\t"
    "v_exp_f32 v228, v64\n\t"
    "v_exp_f32 v229, v65\n\t"
    "v_exp_f32 v230, v66\n\t"
    "v_exp_f32 v231, v67\n\t"
    "s_waitcnt lgkmcnt(4)\n\t"
    "v_mfma_f32_32x32x16_bf16 v[80:95], v[40:43], v[108:111], v[80:95]\n\t"
    "ds_read_b64_tr_b16 v[56:57], v99 offset:32768\n\t"
    "ds_read_b64_tr_b16 v[58:59], v99 offset:33792\n\t"
    "ds_read_b64_tr_b16 v[60:61], v99 offset:36864\n\t"
    "ds_read_b64_tr_b16 v[62:63], v99 offset:37888\n\t"
    "v_exp_f32 v232, v68\n\t"
    "v_exp_f32 v233, v69\n\t"
    "v_exp_f32 v234, v70\n\t"
    "v_exp_f32 v235, v71\n\t"
    "v_mfma_f32_32x32x16_bf16 v[80:95], v[44:47], v[112:115], v[80:95]\n\t"
    "s_barrier\n\t"
    "s_waitcnt lgkmcnt(4)\n\t"
    "v_mfma_f32_32x32x16_bf16 v[80:95], v[48:51], v[116:119], v[80:95]\n\t"
    "ds_read_b64_tr_b16 v[32:33], v96 offset:0\n\t"
    "ds_read_b64_tr_b16 v[34:35], v96 offset:1024\n\t"
    "v_fma_f32 v228, v228, v228, 1.0\n\t"
    "v_fma_f32 v229, v229, v229, 1.0\n\t"
    "v_fma_f32 v230, v230, v230, 1.0\n\t"
    "v_fma_f32 v231, v231, v231, 1.0\n\t"
    "v_fma_f32 v232, v232, v232, 1.0\n\t"
    "v_mfma_f32_32x32x16_bf16 v[80:95], v[52:55], v[120:123], v[80:95]\n\t"
    "ds_read_b64_tr_b16 v[36:37], v96 offset:4096\n\t"
    "ds_read_b64_tr_b16 v[38:39], v96 offset:5120\n\t"
    "v_fma_f32 v233, v233, v233, 1.0\n\t"
    "v_fma_f32 v234, v234, v234, 1.0\n\t"
    "v_fma_f32 v235, v235, v235, 1.0\n\t"
    "v_fma_f32 v228, v228, v228, 1.0\n\t"
    "s_waitcnt lgkmcnt(4)\n\t"
    "v_mfma_f32_32x32x16_bf16 v[80:95], v[56:59], v[124:127], v[80:95]\n\t"
    "ds_read_b64_tr_b16 v[40:41], v97 offset:0\n\t"
    "ds_read_b64_tr_b16 v[42:43], v97 offset:1024\n\t"
    "ds_read_b64_tr_b16 v[44:45], v97 offset:4096\n\t"
    "ds_read_b64_tr_b16 v[46:47], v97 offset:5120\n\t"
    "v_fma_f32 v229, v229, v229, 1.0\n\t"
    "v_fma_f32 v230, v230, v230, 1.0\n\t"
    "v_fma_f32 v231, v231, v231, 1.0\n\t"
    "v_fma_f32 v232, v232, v232, 1.0\n\t"
    "v_fma_f32 v233, v233, v233, 1.0\n\t"
    "v_mfma_f32_32x32x16_bf16 v[80:95], v[60:63], v[128:131], v[80:95]\n\t"
    "s_barrier\n\t"
    "s_waitcnt lgkmcnt(4)\n\t"
    "v_mfma_f32_32x32x16_bf16 v[80:95], v[32:35], v[132:135], v[80:95]\n\t"
    "ds_read_b64_tr_b16 v[48:49], v96 offset:16384\n\t"
    "ds_read_b64_tr_b16 v[50:51], v96 offset:17408\n\t"
    "v_fma_f32 v234, v234, v234, 1.0\n\t"
    "v_fma_f32 v235, v235, v235, 1.0\n\t"
    "v_rcp_f32 v228, v228 mul:2\n\t"
    "v_mfma_f32_32x32x16_bf16 v[80:95], v[36:39], v[136:139], v[80:95]\n\t"
    "ds_read_b64_tr_b16 v[52:53], v96 offset:20480\n\t"
    "ds_read_b64_tr_b16 v[54:55], v96 offset:21504\n\t"
    "v_rcp_f32 v229, v229 mul:2\n\t"
    "v_rcp_f32 v230, v230 mul:2\n\t"
    "v_rcp_f32 v231, v231 mul:2\n\t"
    "s_waitcnt lgkmcnt(4)\n\t"
    "v_mfma_f32_32x32x16_bf16 v[80:95], v[40:43], v[140:143], v[80:95]\n\t"
    "ds_read_b64_tr_b16 v[56:57], v97 offset:16384\n\t"
    "ds_read_b64_tr_b16 v[58:59], v97 offset:17408\n\t"
    "ds_read_b64_tr_b16 v[60:61], v97 offset:20480\n\t"
    "ds_read_b64_tr_b16 v[62:63], v97 offset:21504\n\t"
    "v_rcp_f32 v232, v232 mul:2\n\t"
    "v_rcp_f32 v233, v233 mul:2\n\t"
    "v_rcp_f32 v234, v234 mul:2\n\t"
    "v_mfma_f32_32x32x16_bf16 v[80:95], v[44:47], v[144:147], v[80:95]\n\t"
    "s_barrier\n\t"
    "s_waitcnt lgkmcnt(4)\n\t"
    "v_mfma_f32_32x32x16_bf16 v[80:95], v[48:51], v[148:151], v[80:95]\n\t"
    "ds_read_b64_tr_b16 v[32:33], v98 offset:0\n\t"
    "ds_read_b64_tr_b16 v[34:35], v98 offset:1024\n\t"
    "v_rcp_f32 v235, v235 mul:2\n\t"
    "v_fma_f32 v228, -v64, v228, v64\n\t"
    "v_fma_f32 v229, -v65, v229, v65\n\t"
    "v_fma_f32 v230, -v66, v230, v66\n\t"
    "v_mfma_f32_32x32x16_bf16 v[80:95], v[52:55], v[152:155], v[80:95]\n\t"
    "ds_read_b64_tr_b16 v[36:37], v98 offset:4096\n\t"
    "ds_read_b64_tr_b16 v[38:39], v98 offset:5120\n\t"
    "v_fma_f32 v231, -v67, v231, v67\n\t"
    "v_fma_f32 v232, -v68, v232, v68\n\t"
    "v_fma_f32 v233, -v69, v233, v69\n\t"
    "v_fma_f32 v234, -v70, v234, v70\n\t"
    "s_waitcnt lgkmcnt(4)\n\t"
    "v_mfma_f32_32x32x16_bf16 v[80:95], v[56:59], v[156:159], v[80:95]\n\t"
    "ds_read_b64_tr_b16 v[40:41], v99 offset:0\n\t"
    "ds_read_b64_tr_b16 v[42:43], v99 offset:1024\n\t"
    "ds_read_b64_tr_b16 v[44:45], v99 offset:4096\n\t"
    "ds_read_b64_tr_b16 v[46:47], v99 offset:5120\n\t"
    "v_fma_f32 v235, -v71, v235, v71\n\t"
    "v_cvt_pk_bf16_f32 v236, v228, v229\n\t"
    "v_cvt_pk_bf16_f32 v237, v230, v231\n\t"
    "v_cvt_pk_bf16_f32 v238, v232, v233\n\t"
    "v_cvt_pk_bf16_f32 v239, v234, v235\n\t"
    "v_mfma_f32_32x32x16_bf16 v[80:95], v[60:63], v[160:163], v[80:95]\n\t"
    "s_cbranch_vccnz .Lfmm_sd2\n\t"
    ".Lfmm_bk2:\n\t"
    "s_barrier\n\t"
    "s_waitcnt lgkmcnt(4)\n\t"
    "v_mfma_f32_32x32x16_bf16 v[80:95], v[32:35], v[164:167], v[80:95]\n\t"
    "ds_read_b64_tr_b16 v[48:49], v98 offset:16384\n\t"
    "ds_read_b64_tr_b16 v[50:51], v98 offset:17408\n\t"
    "v_mov_b32 v108, v11\n\t"
    "v_mov_b32 v109, v11\n\t"
    "v_exp_f32 v228, v72\n\t"
    "v_exp_f32 v229, v73\n\t"
    "v_mfma_f32_32x32x16_bf16 v[80:95], v[36:39], v[168:171], v[80:95]\n\t"
    "ds_read_b64_tr_b16 v[52:53], v98 offset:20480\n\t"
    "ds_read_b64_tr_b16 v[54:55], v98 offset:21504\n\t"
    "v_mov_b32 v110, v11\n\t"
    "v_exp_f32 v230, v74\n\t"
    "v_exp_f32 v231, v75\n\t"
    "v_exp_f32 v232, v76\n\t"
    "s_waitcnt lgkmcnt(4)\n\t"
    "v_mfma_f32_32x32x16_bf16 v[80:95], v[40:43], v[172:175], v[80:95]\n\t"
    "ds_read_b64_tr_b16 v[56:57], v99 offset:16384\n\t"
    "ds_read_b64_tr_b16 v[58:59], v99 offset:17408\n\t"
    "ds_read_b64_tr_b16 v[60:61], v99 offset:20480\n\t"
    "ds_read_b64_tr_b16 v[62:63], v99 offset:21504\n\t"
    "v_mov_b32 v111, v11\n\t"
    "v_exp_f32 v233, v77\n\t"
    "v_exp_f32 v234, v78\n\t"
    "v_exp_f32 v235, v79\n\t"
    "v_mfma_f32_32x32x16_bf16 v[80:95], v[44:47], v[176:179], v[80:95]\n\t"
    "s_barrier\n\t"
    "s_waitcnt lgkmcnt(4)\n\t"
    "v_mfma_f32_32x32x16_bf16 v[80:95], v[48:51], v[180:183], v[80:95]\n\t"
    "ds_read_b64_tr_b16 v[32:33], v98 offset:32768\n\t"
    "ds_read_b64_tr_b16 v[34:35], v98 offset:33792\n\t"
    "v_mov_b32 v112, v11\n\t"
    "v_mov_b32 v113, v11\n\t"
    "v_fma_f32 v228, v228, v228, 1.0\n\t"
    "v_fma_f32 v229, v229, v229, 1.0\n\t"
    "v_fma_f32 v230, v230, v230, 1.0\n\t"
    "v_fma_f32 v231, v231, v231, 1.0\n\t"
    "v_fma_f32 v232, v232, v232, 1.0\n\t"
    "v_mfma_f32_32x32x16_bf16 v[80:95], v[52:55], v[184:187], v[80:95]\n\t"
    "ds_read_b64_tr_b16 v[36:37], v98 offset:36864\n\t"
    "ds_read_b64_tr_b16 v[38:39], v98 offset:37888\n\t"
    "v_mov_b32 v114, v11\n\t"
    "v_fma_f32 v233, v233, v233, 1.0\n\t"
    "v_fma_f32 v234, v234, v234, 1.0\n\t"
    "v_fma_f32 v235, v235, v235, 1.0\n\t"
    "v_fma_f32 v228, v228, v228, 1.0\n\t"
    "s_waitcnt lgkmcnt(4)\n\t"
    "v_mfma_f32_32x32x16_bf16 v[80:95], v[56:59], v[188:191], v[80:95]\n\t"
    "ds_read_b64_tr_b16 v[40:41], v99 offset:32768\n\t"
    "ds_read_b64_tr_b16 v[42:43], v99 offset:33792\n\t"
    "ds_read_b64_tr_b16 v[44:45], v99 offset:36864\n\t"
    "ds_read_b64_tr_b16 v[46:47], v99 offset:37888\n\t"
    "v_mov_b32 v115, v11\n\t"
    "v_fma_f32 v229, v229, v229, 1.0\n\t"
    "v_fma_f32 v230, v230, v230, 1.0\n\t"
    "v_fma_f32 v231, v231, v231, 1.0\n\t"
    "v_fma_f32 v232, v232, v232, 1.0\n\t"
    "v_fma_f32 v233, v233, v233, 1.0\n\t"
    "v_mfma_f32_32x32x16_bf16 v[80:95], v[60:63], v[192:195], v[80:95]\n\t"
    "s_barrier\n\t"
    "s_waitcnt lgkmcnt(4)\n\t"
    "v_mfma_f32_32x32x16_bf16 v[80:95], v[32:35], v[196:199], v[80:95]\n\t"
    "ds_read_b64_tr_b16 v[48:49], v96 offset:0\n\t"
    "ds_read_b64_tr_b16 v[50:51], v96 offset:1024\n\t"
    "v_mov_b32 v15, 0xdddddddd\n\t"
    "v_fma_f32 v234, v234, v234, 1.0\n\t"
    "v_fma_f32 v235, v235, v235, 1.0\n\t"
    "v_rcp_f32 v228, v228 mul:2\n\t"
    "v_mfma_f32_32x32x16_bf16 v[80:95], v[36:39], v[200:203], v[80:95]\n\t"
    "ds_read_b64_tr_b16 v[52:53], v96 offset:4096\n\t"
    "ds_read_b64_tr_b16 v[54:55], v96 offset:5120\n\t"
    ".p2align 12\n\t"
    ".Lfmm_page2:\n\t"
    "s_cbranch_execz .Lfmm_ret\n\t"
    "s_mov_b32 s66, 0xffff\n\t"
    "v_rcp_f32 v229, v229 mul:2\n\t"
    "v_rcp_f32 v230, v230 mul:2\n\t"
    "v_rcp_f32 v231, v231 mul:2\n\t"
    "s_waitcnt lgkmcnt(4)\n\t"
    "v_mfma_f32_32x32x16_bf16 v[80:95], v[40:43], v[204:207], v[80:95]\n\t"
    "ds_read_b64_tr_b16 v[56:57], v97 offset:0\n\t"
    "ds_read_b64_tr_b16 v[58:59], v97 offset:1024\n\t"
    "ds_read_b64_tr_b16 v[60:61], v97 offset:4096\n\t"
    "ds_read_b64_tr_b16 v[62:63], v97 offset:5120\n\t"
    "s_mov_b32 s67, 0xffff\n\t"
    "v_rcp_f32 v232, v232 mul:2\n\t"
    "v_rcp_f32 v233, v233 mul:2\n\t"
    "v_rcp_f32 v234, v234 mul:2\n\t"
    "v_mfma_f32_32x32x16_bf16 v[80:95], v[44:47], v[208:211], v[80:95]\n\t"
    "s_barrier\n\t"
    "s_waitcnt lgkmcnt(4)\n\t"
    "v_mfma_f32_32x32x16_bf16 v[80:95], v[48:51], v[212:215], v[80:95]\n\t"
    "ds_read_b64_tr_b16 v[32:33], v254 offset:16384\n\t"
    "ds_read_b64_tr_b16 v[34:35], v254 offset:16640\n\t"
    "v_rcp_f32 v235, v235 mul:2\n\t"
    "v_fma_f32 v228, -v72, v228, v72\n\t"
    "v_fma_f32 v229, -v73, v229, v73\n\t"
    "v_fma_f32 v230, -v74, v230, v74\n\t"
    "v_mfma_f32_32x32x16_bf16 v[80:95], v[52:55], v[216:219], v[80:95]\n\t"
    "ds_read_b64_tr_b16 v[36:37], v254 offset:18432\n\t"
    "ds_read_b64_tr_b16 v[38:39], v254 offset:18688\n\t"
    "v_fma_f32 v231, -v75, v231, v75\n\t"
    "v_fma_f32 v232, -v76, v232, v76\n\t"
    "v_fma_f32 v233, -v77, v233, v77\n\t"
    "v_fma_f32 v234, -v78, v234, v78\n\t"
    "s_waitcnt lgkmcnt(4)\n\t"
    "v_mfma_f32_32x32x16_bf16 v[80:95], v[56:59], v[220:223], v[80:95]\n\t"
    "ds_read_b64_tr_b16 v[40:41], v254 offset:24576\n\t"
    "ds_read_b64_tr_b16 v[42:43], v254 offset:24832\n\t"
    "ds_read_b64_tr_b16 v[44:45], v254 offset:26624\n\t"
    "ds_read_b64_tr_b16 v[46:47], v254 offset:26880\n\t"
    "v_fma_f32 v235, -v79, v235, v79\n\t"
    "v_cvt_pk_bf16_f32 v240, v228, v229\n\t"
    "v_cvt_pk_bf16_f32 v241, v230, v231\n\t"
    "v_cvt_pk_bf16_f32 v242, v232, v233\n\t"
    "v_cvt_pk_bf16_f32 v243, v234, v235\n\t"
    "v_mfma_f32_32x32x16_bf16 v[80:95], v[60:63], v[224:227], v[80:95]\n\t"
    "s_cbranch_vccnz .Lfmm_sd3\n\t"
    ".Lfmm_bk3:\n\t"
    "s_barrier\n\t"
    "s_waitcnt lgkmcnt(4)\n\t"
    "v_mfma_f32_32x32x16_bf16 v[64:79], v[32:35], v[100:103], 0\n\t"
    "ds_read_b64_tr_b16 v[48:49], v255 offset:0\n\t"
    "ds_read_b64_tr_b16 v[50:51], v255 offset:256\n\t"
    "v_mfma_f32_32x32x16_bf16 v[64:79], v[36:39], v[104:107], v[64:79]\n\t"
    "ds_read_b64_tr_b16 v[52:53], v255 offset:2048\n\t"
    "ds_read_b64_tr_b16 v[54:55], v255 offset:2304\n\t"
    "s_nop 2\n\t"
    "v_exp_f32 v228, v80\n\t"
    "v_exp_f32 v229, v81\n\t"
    "v_exp_f32 v230, v82\n\t"
    "v_exp_f32 v231, v83\n\t"
    "v_exp_f32 v232, v84\n\t"
    "v_exp_f32 v233, v85\n\t"
    "s_waitcnt lgkmcnt(4)\n\t"
    "v_mfma_f32_32x32x16_bf16 v[64:79], v[40:43], v[116:119], v[64:79]\n\t"
    "ds_read_b64_tr_b16 v[56:57], v255 offset:8192\n\t"
    "ds_read_b64_tr_b16 v[58:59], v255 offset:8448\n\t"
    "ds_read_b64_tr_b16 v[60:61], v255 offset:10240\n\t"
    "ds_read_b64_tr_b16 v[62:63], v255 offset:10496\n\t"
    "v_exp_f32 v234, v86\n\t"
    "v_exp_f32 v235, v87\n\t"
    "v_fma_f32 v228, v228, v228, 1.0\n\t"
    "v_fma_f32 v229, v229, v229, 1.0\n\t"
    "v_fma_f32 v230, v230, v230, 1.0\n\t"
    "v_fma_f32 v231, v231, v231, 1.0\n\t"
    "v_fma_f32 v232, v232, v232, 1.0\n\t"
    "v_fma_f32 v233, v233, v233, 1.0\n\t"
    "v_fma_f32 v234, v234, v234, 1.0\n\t"
    "v_fma_f32 v235, v235, v235, 1.0\n\t"
    "v_fma_f32 v228, v228, v228, 1.0\n\t"
    "v_fma_f32 v229, v229, v229, 1.0\n\t"
    "s_cmp_eq_u32 s44, 0\n\t"
    "s_cbranch_scc1 .Lfmm_sd4\n\t"
    ".Lfmm_bk4:\n\t"
    "v_mfma_f32_32x32x16_bf16 v[64:79], v[44:47], v[120:123], v[64:79]\n\t"
    "s_barrier\n\t"
    "s_waitcnt lgkmcnt(4)\n\t"
    "v_mfma_f32_32x32x16_bf16 v[64:79], v[48:51], v[132:135], v[64:79]\n\t"
    "ds_read_b64_tr_b16 v[32:33], v255 offset:16384\n\t"
    "ds_read_b64_tr_b16 v[34:35], v255 offset:16640\n\t"
    "v_fma_f32 v230, v230, v230, 1.0\n\t"
    "v_fma_f32 v231, v231, v231, 1.0\n\t"
    "v_fma_f32 v232, v232, v232, 1.0\n\t"
    "v_fma_f32 v233, v233, v233, 1.0\n\t"
    "v_fma_f32 v234, v234, v234, 1.0\n\t"
    "v_fma_f32 v235, v235, v235, 1.0\n\t"
    "v_rcp_f32 v228, v228 mul:2\n\t"
    "v_rcp_f32 v229, v229 mul:2\n\t"
    "v_mfma_f32_32x32x16_bf16 v[64:79], v[52:55], v[136:139], v[64:79]\n\t"
    "ds_read_b64_tr_b16 v[36:37], v255 offset:18432\n\t"
    "ds_read_b64_tr_b16 v[38:39], v255 offset:18688\n\t"
    "v_mov_b32 v104, 0\n\t"
    "v_mov_b32 v105, 0\n\t"
    "v_rcp_f32 v230, v230 mul:2\n\t"
    "v_rcp_f32 v231, v231 mul:2\n\t"
    "v_rcp_f32 v232, v232 mul:2\n\t"
    "v_rcp_f32 v233, v233 mul:2\n\t"
    "v_rcp_f32 v234, v234 mul:2\n\t"
    "s_waitcnt lgkmcnt(4)\n\t"
    "v_mfma_f32_32x32x16_bf16 v[64:79], v[56:59], v[148:151], v[64:79]\n\t"
    "ds_read_b64_tr_b16 v[40:41], v255 offset:24576\n\t"
    "ds_read_b64_tr_b16 v[42:43], v255 offset:24832\n\t"
    "ds_read_b64_tr_b16 v[44:45], v255 offset:26624\n\t"
    "ds_read_b64_tr_b16 v[46:47], v255 offset:26880\n\t"
    "v_rcp_f32 v235, v235 mul:2\n\t"
    "v_fma_f32 v228, -v80, v228, v80\n\t"
    "v_fma_f32 v229, -v81, v229, v81\n\t"
    "v_fma_f32 v230, -v82, v230, v82\n\t"
    "v_fma_f32 v231, -v83, v231, v83\n\t"
    "v_fma_f32 v232, -v84, v232, v84\n\t"
    "v_fma_f32 v233, -v85, v233, v85\n\t"
    "v_fma_f32 v234, -v86, v234, v86\n\t"
    "v_fma_f32 v235, -v87, v235, v87\n\t"
    "v_mfma_f32_32x32x16_bf16 v[64:79], v[60:63], v[152:155], v[64:79]\n\t"
    "s_barrier\n\t"
    "s_waitcnt lgkmcnt(4)\n\t"
    "v_mfma_f32_32x32x16_bf16 v[64:79], v[32:35], v[164:167], v[64:79]\n\t"
    "ds_read_b64_tr_b16 v[48:49], v255 offset:32768\n\t"
    "ds_read_b64_tr_b16 v[50:51], v255 offset:33024\n\t"
    "v_cvt_pk_bf16_f32 v244, v228, v229\n\t"
    "v_cvt_pk_bf16_f32 v245, v230, v231\n\t"
    "v_cvt_pk_bf16_f32 v246, v232, v233\n\t"
    "v_cvt_pk_bf16_f32 v247, v234, v235\n\t"
    "v_exp_f32 v228, v88\n\t"
    "v_exp_f32 v229, v89\n\t"
    "v_mfma_f32_32x32x16_bf16 v[64:79], v[36:39], v[168:171], v[64:79]\n\t"
    "ds_read_b64_tr_b16 v[52:53], v255 offset:34816\n\t"
    "ds_read_b64_tr_b16 v[54:55], v255 offset:35072\n\t"
    "v_mfma_f32_32x32x8_bf16 v[116:131], v[2:3], v[104:105], 0\n\t"
    "v_exp_f32 v230, v90\n\t"
    "v_exp_f32 v231, v91\n\t"
    "v_exp_f32 v232, v92\n\t"
    "v_exp_f32 v233, v93\n\t"
    "v_exp_f32 v234, v94\n\t"
    "s_waitcnt lgkmcnt(4)\n\t"
    "v_mfma_f32_32x32x16_bf16 v[64:79], v[40:43], v[180:183], v[64:79]\n\t"
    "ds_read_b64_tr_b16 v[56:57], v255 offset:40960\n\t"
    "ds_read_b64_tr_b16 v[58:59], v255 offset:41216\n\t"
    "ds_read_b64_tr_b16 v[60:61], v255 offset:43008\n\t"
    "ds_read_b64_tr_b16 v[62:63], v255 offset:43264\n\t"
    "v_exp_f32 v235, v95\n\t"
    "v_fma_f32 v228, v228, v228, 1.0\n\t"
    "v_fma_f32 v229, v229, v229, 1.0\n\t"
    "v_fma_f32 v230, v230, v230, 1.0\n\t"
    "v_fma_f32 v231, v231, v231, 1.0\n\t"
    "v_fma_f32 v232, v232, v232, 1.0\n\t"
    "v_fma_f32 v233, v233, v233, 1.0\n\t"
    "v_fma_f32 v234, v234, v234, 1.0\n\t"
    "v_fma_f32 v235, v235, v235, 1.0\n\t"
    "v_mfma_f32_32x32x16_bf16 v[64:79], v[44:47], v[184:187], v[64:79]\n\t"
    "s_waitcnt lgkmcnt(4)\n\t"
    "v_mfma_f32_32x32x16_bf16 v[64:79], v[48:51], v[196:199], v[64:79]\n\t"
    "v_fma_f32 v228, v228, v228, 1.0\n\t"
    "v_fma_f32 v229, v229, v229, 1.0\n\t"
    "buffer_store_dwordx4 v[236:239], v4, s[20:23], s16 offen offset:0 sc1\n\t"
    "v_mfma_f32_32x32x16_bf16 v[64:79], v[52:55], v[200:203], v[64:79]\n\t"
    "v_fma_f32 v230, v230, v230, 1.0\n\t"
    "v_fma_f32 v231, v231, v231, 1.0\n\t"
    "buffer_store_dwordx4 v[240:243], v4, s[20:23], s16 offen offset:2048 sc1\n\t"
    "s_waitcnt lgkmcnt(0)\n\t"
    "v_mfma_f32_32x32x16_bf16 v[64:79], v[56:59], v[212:215], v[64:79]\n\t"
    "v_fma_f32 v232, v232, v232, 1.0\n\t"
    "v_fma_f32 v233, v233, v233, 1.0\n\t"
    "v_mfma_f32_32x32x16_bf16 v[64:79], v[60:63], v[216:219], v[64:79]\n\t"
    "s_xor_b32 s56, s44, 2\n\t"
    "s_mul_i32 s55, s44, 3072\n\t"
    "s_mul_i32 s56, s56, 3072\n\t"
    "v_fma_f32 v234, v234, v234, 1.0\n\t"
    "v_fma_f32 v235, v235, v235, 1.0\n\t"
    "v_rcp_f32 v228, v228 mul:2\n\t"
    "v_rcp_f32 v229, v229 mul:2\n\t"
    "v_rcp_f32 v230, v230 mul:2\n\t"
    "v_rcp_f32 v231, v231 mul:2\n\t"
    "v_rcp_f32 v232, v232 mul:2\n\t"
    "v_rcp_f32 v233, v233 mul:2\n\t"
    "v_rcp_f32 v234, v234 mul:2\n\t"
    "v_rcp_f32 v235, v235 mul:2\n\t"
    "ds_write_b128 v28, v[64:67] offset:0\n\t"
    "ds_write_b128 v28, v[68:71] offset:1024\n\t"
    "ds_write_b128 v28, v[72:75] offset:16384\n\t"
    "ds_write_b128 v28, v[76:79] offset:17408\n\t"
    "v_fma_f32 v228, -v88, v228, v88\n\t"
    "v_fma_f32 v229, -v89, v229, v89\n\t"
    "v_fma_f32 v230, -v90, v230, v90\n\t"
    "v_fma_f32 v231, -v91, v231, v91\n\t"
    "s_waitcnt lgkmcnt(0)\n\t"
    "s_barrier\n\t"
    "s_setprio 3\n\t"
    "ds_read_b128 v[48:51], v29 offset:0\n\t"
    "ds_read_b128 v[52:55], v29 offset:1024\n\t"
    "ds_read_b128 v[100:103], v14 offset:0\n\t"
    "ds_read_b128 v[56:59], v29 offset:16384\n\t"
    "ds_read_b128 v[60:63], v29 offset:17408\n\t"
    "s_mov_b64 exec, s[66:67]\n\t"
    "ds_read_b128 v[108:111], v5 offset:0\n\t"
    "ds_read_b128 v[112:115], v5 offset:512\n\t"
    "s_mov_b64 exec, -1\n\t"
    "v_mfma_f32_32x32x8_bf16 v[16:31], v[2:3], v[116:117], 0\n\t"
    "v_fma_f32 v232, -v92, v232, v92\n\t"
    "v_fma_f32 v233, -v93, v233, v93\n\t"
    "v_fma_f32 v234, -v94, v234, v94\n\t"
    "v_fma_f32 v235, -v95, v235, v95\n\t"
    "v_cvt_pk_bf16_f32 v248, v228, v229\n\t"
    "v_cvt_pk_bf16_f32 v249, v230, v231\n\t"
    "s_waitcnt lgkmcnt(5)\n\t"
    "v_pk_add_f32 v[72:73], v[72:73], v[48:49]\n\t"
    "v_pk_add_f32 v[74:75], v[74:75], v[50:51]\n\t"
    "v_pk_add_f32 v[76:77], v[76:77], v[52:53]\n\t"
    "v_pk_add_f32 v[78:79], v[78:79], v[54:55]\n\t"
    "v_cvt_pk_bf16_f32 v104, v72, v73\n\t"
    "v_cvt_pk_bf16_f32 v105, v74, v75\n\t"
    "v_cvt_pk_bf16_f32 v106, v76, v77\n\t"
    "v_cvt_pk_bf16_f32 v107, v78, v79\n\t"
    "s_waitcnt lgkmcnt(4)\n\t"
    "s_nop 0\n\t"
    "v_mfma_f32_32x32x16_bf16 v[32:47], v[100:103], v[104:107], -4.0\n\t"
    "s_waitcnt lgkmcnt(2)\n\t"
    "v_pk_add_f32 v[64:65], v[64:65], v[56:57]\n\t"
    "v_pk_add_f32 v[66:67], v[66:67], v[58:59]\n\t"
    "v_pk_add_f32 v[68:69], v[68:69], v[60:61]\n\t"
    "v_pk_add_f32 v[70:71], v[70:71], v[62:63]\n\t"
    "v_cvt_pk_bf16_f32 v96, v64, v65\n\t"
    "v_cvt_pk_bf16_f32 v97, v66, v67\n\t"
    "v_cvt_pk_bf16_f32 v98, v68, v69\n\t"
    "v_cvt_pk_bf16_f32 v99, v70, v71\n\t"
    "v_cvt_pk_bf16_f32 v250, v232, v233\n\t"
    "v_cvt_pk_bf16_f32 v251, v234, v235\n\t"
    "v_add_u32 v2, s55, v4\n\t"
    "v_add_u32 v3, s56, v4\n\t"
    "v_mov_b32 v132, -1\n\t"
    "v_mov_b32 v133, -1\n\t"
    "v_mov_b32 v134, -1\n\t"
    "v_mov_b32 v135, -1\n\t"
    "s_mul_i32 s60, s2, 7\n\t"
    "v_min_u32 v140, 0x60, v4\n\t"
    "v_and_b32 v141, 16, v4\n\t"
    "v_lshlrev_b32 v140, 3, v140\n\t"
    "v_lshlrev_b32 v141, 3, v141\n\t"
    "v_lshl_add_u32 v140, s60, 7, v140\n\t"
    "v_lshl_add_u32 v141, s2, 8, v141\n\t"
    "s_branch .Lfmm_headend\n\t"
    ".Lfmm_roleb:\n\t"
    "s_setprio 1\n\t"
    "s_add_u32 s60, s61, 192\n\t"
    "s_and_b32 s60, s60, 511\n\t"
    "s_mul_i32 s60, s60, 3584\n\t"
    "s_add_u32 m0, s69, 114688\n\t"
    "s_add_u32 s60, s60, s47\n\t"
    "buffer_load_dwordx4 v236, s[8:11], s60 offen offset:0 lds\n\t"
    "buffer_load_dwordx4 v237, s[8:11], s60 offen offset:1024 lds\n\t"
    "buffer_load_dwordx4 v238, s[8:11], s60 offen offset:2048 lds\n\t"
    "buffer_load_dwordx4 v239, s[8:11], s60 offen offset:3072 lds\n\t"
    "s_add_u32 s60, s61, 208\n\t"
    "s_and_b32 s60, s60, 511\n\t"
    "s_mul_i32 s60, s60, 3584\n\t"
    "s_add_u32 m0, s69, 118784\n\t"
    "s_add_u32 s60, s60, s47\n\t"
    "buffer_load_dwordx4 v236, s[8:11], s60 offen offset:0 lds\n\t"
    "buffer_load_dwordx4 v237, s[8:11], s60 offen offset:1024 lds\n\t"
    "buffer_load_dwordx4 v238, s[8:11], s60 offen offset:2048 lds\n\t"
    "buffer_load_dwordx4 v239, s[8:11], s60 offen offset:3072 lds\n\t"
    "s_waitcnt vmcnt(12)\n\t"
    "s_barrier\n\t"
    "ds_read_b64_tr_b16 v[164:165], v22 offset:0\n\t"
    "ds_read_b64_tr_b16 v[166:167], v22 offset:128\n\t"
    "ds_read_b128 v[168:171], v23 offset:0\n\t"
    "s_add_u32 s60, s61, 256\n\t"
    "s_and_b32 s60, s60, 511\n\t"
    "s_mul_i32 s60, s60, 3584\n\t"
    "s_add_u32 m0, s69, 131072\n\t"
    "s_add_u32 s60, s60, s47\n\t"
    "buffer_load_dwordx4 v236, s[8:11], s60 offen offset:0 lds\n\t"
    "ds_read_b64_tr_b16 v[172:173], v22 offset:1024\n\t"
    "ds_read_b64_tr_b16 v[174:175], v22 offset:1152\n\t"
    "ds_read_b128 v[176:179], v24 offset:4096\n\t"
    "buffer_load_dwordx4 v237, s[8:11], s60 offen offset:1024 lds\n\t"
    "ds_read_b64_tr_b16 v[180:181], v22 offset:2048\n\t"
    "ds_read_b64_tr_b16 v[182:183], v22 offset:2176\n\t"
    "ds_read_b128 v[184:187], v23 offset:8192\n\t"
    "buffer_load_dwordx4 v238, s[8:11], s60 offen offset:2048 lds\n\t"
    "ds_read_b64_tr_b16 v[188:189], v22 offset:3072\n\t"
    "ds_read_b64_tr_b16 v[190:191], v22 offset:3200\n\t"
    "ds_read_b128 v[192:195], v24 offset:12288\n\t"
    "buffer_load_dwordx4 v239, s[8:11], s60 offen offset:3072 lds\n\t"
    "ds_read_b64_tr_b16 v[196:197], v22 offset:4096\n\t"
    "ds_read_b64_tr_b16 v[198:199], v22 offset:4224\n\t"
    "ds_read_b128 v[200:203], v23 offset:16384\n\t"
    "s_add_u32 s60, s61, 272\n\t"
    "s_and_b32 s60, s60, 511\n\t"
    "s_mul_i32 s60, s60, 3584\n\t"
    "s_add_u32 m0, s69, 135168\n\t"
    "s_add_u32 s60, s60, s47\n\t"
    "buffer_load_dwordx4 v236, s[8:11], s60 offen offset:0 lds\n\t"
    "ds_read_b64_tr_b16 v[204:205], v22 offset:5120\n\t"
    "ds_read_b64_tr_b16 v[206:207], v22 offset:5248\n\t"
    "ds_read_b128 v[208:211], v24 offset:20480\n\t"
    "buffer_load_dwordx4 v237, s[8:11], s60 offen offset:1024 lds\n\t"
    "ds_read_b64_tr_b16 v[212:213], v22 offset:6144\n\t"
    "ds_read_b64_tr_b16 v[214:215], v22 offset:6272\n\t"
    "ds_read_b128 v[216:219], v23 offset:24576\n\t"
    "buffer_load_dwordx4 v238, s[8:11], s60 offen offset:2048 lds\n\t"
    "ds_read_b64_tr_b16 v[220:221], v22 offset:7168\n\t"
    "ds_read_b64_tr_b16 v[222:223], v22 offset:7296\n\t"
    "ds_read_b128 v[224:227], v24 offset:28672\n\t"
    "buffer_load_dwordx4 v239, s[8:11], s60 offen offset:3072 lds\n\t"
    "s_waitcnt vmcnt(16)\n\t"
    "s_barrier\n\t"
    "s_cmp_lt_u32 s45, 4\n\t"
    "s_cbranch_scc0 14f\n\t"
    "s_waitcnt lgkmcnt(15)\n\t"
    "v_mfma_f32_16x16x32_bf16 v[228:231], v[164:167], v[168:171], 0\n\t"
    "v_mfma_f32_16x16x32_bf16 v[228:231], v[172:175], v[176:179], v[228:231]\n\t"
    "v_mfma_f32_16x16x32_bf16 v[228:231], v[180:183], v[184:187], v[228:231]\n\t"
    "s_waitcnt lgkmcnt(9)\n\t"
    "v_mfma_f32_16x16x32_bf16 v[228:231], v[188:191], v[192:195], v[228:231]\n\t"
    "v_mfma_f32_16x16x32_bf16 v[228:231], v[196:199], v[200:203], v[228:231]\n\t"
    "s_waitcnt lgkmcnt(3)\n\t"
    "v_mfma_f32_16x16x32_bf16 v[228:231], v[204:207], v[208:211], v[228:231]\n\t"
    "v_mfma_f32_16x16x32_bf16 v[228:231], v[212:215], v[216:219], v[228:231]\n\t"
    "s_waitcnt lgkmcnt(0)\n\t"
    "v_mfma_f32_16x16x32_bf16 v[228:231], v[220:223], v[224:227], v[228:231]\n\t"
    "s_branch 15f\n\t"
    "14:\n\t"
    "s_waitcnt lgkmcnt(15)\n\t"
    "v_mfma_f32_16x16x32_bf16 v[228:231], v[168:171], v[164:167], 0\n\t"
    "v_mfma_f32_16x16x32_bf16 v[228:231], v[176:179], v[172:175], v[228:231]\n\t"
    "v_mfma_f32_16x16x32_bf16 v[228:231], v[184:187], v[180:183], v[228:231]\n\t"
    "s_waitcnt lgkmcnt(9)\n\t"
    "v_mfma_f32_16x16x32_bf16 v[228:231], v[192:195], v[188:191], v[228:231]\n\t"
    "v_mfma_f32_16x16x32_bf16 v[228:231], v[200:203], v[196:199], v[228:231]\n\t"
    "s_waitcnt lgkmcnt(3)\n\t"
    "v_mfma_f32_16x16x32_bf16 v[228:231], v[208:211], v[204:207], v[228:231]\n\t"
    "v_mfma_f32_16x16x32_bf16 v[228:231], v[216:219], v[212:215], v[228:231]\n\t"
    "s_waitcnt lgkmcnt(0)\n\t"
    "v_mfma_f32_16x16x32_bf16 v[228:231], v[224:227], v[220:223], v[228:231]\n\t"
    "15:\n\t"
    "s_add_u32 s60, s61, 320\n\t"
    "s_and_b32 s60, s60, 511\n\t"
    "s_mul_i32 s60, s60, 3584\n\t"
    "s_add_u32 m0, s69, 0\n\t"
    "s_add_u32 s60, s60, s47\n\t"
    "buffer_load_dwordx4 v236, s[8:11], s60 offen offset:0 lds\n\t"
    "buffer_load_dwordx4 v237, s[8:11], s60 offen offset:1024 lds\n\t"
    "s_nop 0\n\t"
    "ds_write_b128 v20, v[228:231]\n\t"
    "buffer_load_dwordx4 v238, s[8:11], s60 offen offset:2048 lds\n\t"
    "buffer_load_dwordx4 v239, s[8:11], s60 offen offset:3072 lds\n\t"
    "s_add_u32 s60, s61, 336\n\t"
    "s_and_b32 s60, s60, 511\n\t"
    "s_mul_i32 s60, s60, 3584\n\t"
    "s_add_u32 m0, s69, 4096\n\t"
    "s_add_u32 s60, s60, s47\n\t"
    "buffer_load_dwordx4 v236, s[8:11], s60 offen offset:0 lds\n\t"
    "buffer_load_dwordx4 v237, s[8:11], s60 offen offset:1024 lds\n\t"
    "buffer_load_dwordx4 v238, s[8:11], s60 offen offset:2048 lds\n\t"
    "buffer_load_dwordx4 v239, s[8:11], s60 offen offset:3072 lds\n\t"
    "s_waitcnt vmcnt(16) lgkmcnt(0)\n\t"
    "s_barrier\n\t"
    "s_add_u32 s60, s61, 384\n\t"
    "s_and_b32 s60, s60, 511\n\t"
    "s_mul_i32 s60, s60, 3584\n\t"
    "s_add_u32 m0, s69, 16384\n\t"
    "s_add_u32 s60, s60, s47\n\t"
    "buffer_load_dwordx4 v236, s[8:11], s60 offen offset:0 lds\n\t"
    "buffer_load_dwordx4 v237, s[8:11], s60 offen offset:1024 lds\n\t"
    "v_add_u32 v98, 98304, v96\n\t"
    "v_add_u32 v99, 98304, v97\n\t"
    "s_setreg_imm32_b32 hwreg(HW_REG_MODE, 4, 6), 0x1c\n\t"
    "s_lshl3_add_u32 s99, s94, s45\n\t"
    "s_mov_b32 s19, 131072\n\t"
    "s_lshl3_add_u32 s94, s99, s19\n\t"
    "s_lshl3_add_u32 s3, s99, s44\n\t"
    "buffer_load_dwordx4 v238, s[8:11], s60 offen offset:2048 lds\n\t"
    "buffer_load_dwordx4 v239, s[8:11], s60 offen offset:3072 lds\n\t"
    "s_mul_i32 s64, s99, 28\n\t"
    "s_brev_b32 s62, s49\n\t"
    "s_lshr_b32 s62, s62, 30\n\t"
    "s_lshl1_add_u32 s63, s99, s98\n\t"
    "s_lshl2_add_u32 s63, s63, s62\n\t"
    "s_lshl_b32 s63, s63, 10\n\t"
    "s_add_u32 s99, s63, 262144\n\t"
    "s_add_u32 s60, s61, 400\n\t"
    "s_and_b32 s60, s60, 511\n\t"
    "s_mul_i32 s60, s60, 3584\n\t"
    "s_add_u32 m0, s69, 20480\n\t"
    "s_add_u32 s60, s60, s47\n\t"
    "buffer_load_dwordx4 v236, s[8:11], s60 offen offset:0 lds\n\t"
    "buffer_load_dwordx4 v237, s[8:11], s60 offen offset:1024 lds\n\t"
    "s_mul_i32 s62, s66, 0x5851f42d\n\t"
    "s_mul_hi_u32 s63, s66, 0x4c957f2d\n\t"
    "s_mul_i32 s67, s67, 0x4c957f2d\n\t"
    "s_add_u32 s62, s62, s63\n\t"
    "s_mul_i32 s66, s66, 0x4c957f2d\n\t"
    "s_add_u32 s67, s67, s62\n\t"
    "buffer_load_dwordx4 v238, s[8:11], s60 offen offset:2048 lds\n\t"
    "buffer_load_dwordx4 v239, s[8:11], s60 offen offset:3072 lds\n\t"
    "s_waitcnt vmcnt(16)\n\t"
    "s_barrier\n\t"
    "s_add_u32 s60, s61, 448\n\t"
    "s_and_b32 s60, s60, 511\n\t"
    "s_mul_i32 s60, s60, 3584\n\t"
    "s_add_u32 m0, s69, 98304\n\t"
    "s_add_u32 s60, s60, s47\n\t"
    "buffer_load_dwordx4 v236, s[8:11], s60 offen offset:0 lds\n\t"
    "buffer_load_dwordx4 v237, s[8:11], s60 offen offset:1024 lds\n\t"
    "s_add_u32 s66, s66, 0xf767814f\n\t"
    "s_addc_u32 s67, s67, 0x14057b7e\n\t"
    "v_mov_b32 v12, s66\n\t"
    "v_mov_b32 v13, s67\n\t"
    "s_lshl3_add_u32 s3, s3, 163840\n\t"
    "s_add_u32 s64, s64, s98\n\t"
    "s_add_u32 s64, s64, 4096\n\t"
    "s_lshl2_add_u32 s16, s16, s64\n\t"
    "buffer_load_dwordx4 v238, s[8:11], s60 offen offset:2048 lds\n\t"
    "buffer_load_dwordx4 v239, s[8:11], s60 offen offset:3072 lds\n\t"
    "s_lshl2_add_u32 s17, s17, s64\n\t"
    "s_lshl_b32 s16, s16, 10\n\t"
    "s_lshl_b32 s17, s17, 10\n\t"
    "s_lshl_b32 s62, s96, 8\n\t"
    "s_sub_u32 s96, 0, s62\n\t"
    "v_bfe_u32 v1, v0, 5, 1\n\t"
    "v_lshl_or_b32 v10, v1, 10, v26\n\t"
    "s_add_u32 s60, s61, 464\n\t"
    "s_and_b32 s60, s60, 511\n\t"
    "s_mul_i32 s60, s60, 3584\n\t"
    "s_add_u32 m0, s69, 102400\n\t"
    "s_add_u32 s60, s60, s47\n\t"
    "buffer_load_dwordx4 v236, s[8:11], s60 offen offset:0 lds\n\t"
    "buffer_load_dwordx4 v237, s[8:11], s60 offen offset:1024 lds\n\t"
    "s_and_b32 s62, s44, 2\n\t"
    "s_and_b32 s64, s44, 1\n\t"
    "s_lshl2_add_u32 s64, s62, s64\n\t"
    "s_lshl_b32 s65, s62, 4\n\t"
    "v_xor_b32 v254, s65, v10\n\t"
    "v_lshl_add_u32 v254, s64, 9, v254\n\t"
    "buffer_load_dwordx4 v238, s[8:11], s60 offen offset:2048 lds\n\t"
    "buffer_load_dwordx4 v239, s[8:11], s60 offen offset:3072 lds\n\t"
    "s_waitcnt vmcnt(16)\n\t"
    "s_barrier\n\t"
    "s_add_u32 s60, s61, 0\n\t"
    "s_and_b32 s60, s60, 511\n\t"
    "s_mul_i32 s60, s60, 3584\n\t"
    "s_add_u32 m0, s69, 114688\n\t"
    "s_add_u32 s60, s60, s48\n\t"
    "buffer_load_dwordx4 v236, s[8:11], s60 offen offset:0 lds\n\t"
    "buffer_load_dwordx4 v237, s[8:11], s60 offen offset:1024 lds\n\t"
    "v_add_u32 v255, 98304, v254\n\t"
    "s_add_u32 s62, s46, 131072\n\t"
    "v_add_u32 v10, s62, v10\n\t"
    "v_and_b32 v5, 0xf0, v4\n\t"
    "v_lshl_or_b32 v5, v1, 8, v5\n\t"
    "s_and_b32 s62, s44, 2\n\t"
    "s_lshl_b32 s62, s62, 10\n\t"
    "buffer_load_dwordx4 v238, s[8:11], s60 offen offset:2048 lds\n\t"
    "buffer_load_dwordx4 v239, s[8:11], s60 offen offset:3072 lds\n\t"
    "s_add_u32 s62, s62, 32768\n\t"
    "v_add_u32 v14, s62, v4\n\t"
    "s_add_u32 s62, s62, 0x400\n\t"
    "v_add_u32 v5, s62, v5\n\t"
    "v_and_b32 v1, 31, v0\n\t"
    "v_cmp_eq_u32 s[64:65], 16, v1\n\t"
    "v_mov_b32 v1, 0x3f003f00\n\t"
    "s_add_u32 s60, s61, 16\n\t"
    "s_and_b32 s60, s60, 511\n\t"
    "s_mul_i32 s60, s60, 3584\n\t"
    "s_add_u32 m0, s69, 118784\n\t"
    "s_add_u32 s60, s60, s48\n\t"
    "buffer_load_dwordx4 v236, s[8:11], s60 offen offset:0 lds\n\t"
    "buffer_load_dwordx4 v237, s[8:11], s60 offen offset:1024 lds\n\t"
    "v_add_u32 v29, s46, v4\n\t"
    "v_cndmask_b32 v11, 0, v1, s[64:65]\n\t"
    "v_xor_b32 v28, 0x1000, v29\n\t"
    "s_and_b32 s68, s2, 7\n\t"
    "s_lshr_b32 s62, s2, 6\n\t"
    "s_lshl2_add_u32 s58, s68, s62\n\t"
    "buffer_load_dwordx4 v238, s[8:11], s60 offen offset:2048 lds\n\t"
    "buffer_load_dwordx4 v239, s[8:11], s60 offen offset:3072 lds\n\t"
    "s_waitcnt vmcnt(16)\n\t"
    "s_barrier\n\t"
    "s_add_u32 s60, s61, 64\n\t"
    "s_and_b32 s60, s60, 511\n\t"
    "s_mul_i32 s60, s60, 3584\n\t"
    "s_add_u32 m0, s69, 131072\n\t"
    "s_add_u32 s60, s60, s48\n\t"
    "buffer_load_dwordx4 v236, s[8:11], s60 offen offset:0 lds\n\t"
    "buffer_load_dwordx4 v237, s[8:11], s60 offen offset:1024 lds\n\t"
    "s_bfe_u32 s59, s2, 0x30003\n\t"
    "s_lshl3_add_u32 s63, s58, s44\n\t"
    "s_mul_i32 s62, s44, 229376\n\t"
    "s_add_u32 s36, s36, s62\n\t"
    "s_addc_u32 s37, s37, 0\n\t"
    "s_lshl_b32 s62, s44, 16\n\t"
    "s_add_u32 s32, s32, s62\n\t"
    "s_addc_u32 s33, s33, 0\n\t"
    "s_mul_i32 s62, s63, 28672\n\t"
    "buffer_load_dwordx4 v238, s[8:11], s60 offen offset:2048 lds\n\t"
    "buffer_load_dwordx4 v239, s[8:11], s60 offen offset:3072 lds\n\t"
    "s_add_u32 s62, s62, 4198400\n\t"
    "s_add_u32 s52, s20, s62\n\t"
    "s_addc_u32 s53, s21, 0\n\t"
    "s_lshl_b32 s57, s63, 13\n\t"
    "s_lshl_b32 s63, s63, 6\n\t"
    "s_add_u32 s57, s57, 262144\n\t"
    "s_add_u32 s63, s63, 163840\n\t"
    "v_and_b32 v1, 7, v0\n\t"
    "s_add_u32 s60, s61, 80\n\t"
    "s_and_b32 s60, s60, 511\n\t"
    "s_mul_i32 s60, s60, 3584\n\t"
    "s_add_u32 m0, s69, 135168\n\t"
    "s_add_u32 s60, s60, s48\n\t"
    "buffer_load_dwordx4 v236, s[8:11], s60 offen offset:0 lds\n\t"
    "buffer_load_dwordx4 v237, s[8:11], s60 offen offset:1024 lds\n\t"
    "v_lshl_add_u32 v7, v1, 3, s63\n\t"
    "v_lshl_add_u32 v8, s59, 7, v27\n\t"
    "v_add_u32 v9, 7168, v8\n\t"
    "s_cmp_gt_u32 s44, 3\n\t"
    "s_cselect_b32 s51, s57, 0x7ff00000\n\t"
    "s_lshr_b32 s62, s2, 3\n\t"
    "s_lshl1_add_u32 s54, s62, s44\n\t"
    "buffer_load_dwordx4 v238, s[8:11], s60 offen offset:2048 lds\n\t"
    "buffer_load_dwordx4 v239, s[8:11], s60 offen offset:3072 lds\n\t"
    "s_waitcnt vmcnt(16)\n\t"
    "s_barrier\n\t"
    "s_add_u32 s60, s61, 128\n\t"
    "s_and_b32 s60, s60, 511\n\t"
    "s_mul_i32 s60, s60, 3584\n\t"
    "s_add_u32 m0, s69, 0\n\t"
    "s_add_u32 s60, s60, s48\n\t"
    "buffer_load_dwordx4 v236, s[8:11], s60 offen offset:0 lds\n\t"
    "buffer_load_dwordx4 v237, s[8:11], s60 offen offset:1024 lds\n\t"
    "s_add_u32 s1, s54, 16\n\t"
    "s_lshl_b32 s1, s1, 11\n\t"
    "s_lshl3_add_u32 s62, s58, s44\n\t"
    "v_and_b32 v1, 7, v0\n\t"
    "v_lshl_add_u32 v1, s62, 3, v1\n\t"
    "buffer_load_dwordx4 v238, s[8:11], s60 offen offset:2048 lds\n\t"
    "buffer_load_dwordx4 v239, s[8:11], s60 offen offset:3072 lds\n\t"
    "v_lshlrev_b32 v1, 7, v1\n\t"
    "v_lshl_or_b32 v1, s59, 4, v1\n\t"
    "v_bfe_u32 v26, v0, 4, 2\n\t"
    "v_and_or_b32 v26, v0, 8, v26\n\t"
    "v_add_lshl_u32 v1, v1, v26, 3\n\t"
    "s_add_u32 s60, s61, 144\n\t"
    "s_and_b32 s60, s60, 511\n\t"
    "s_mul_i32 s60, s60, 3584\n\t"
    "s_add_u32 m0, s69, 4096\n\t"
    "s_add_u32 s60, s60, s48\n\t"
    "buffer_load_dwordx4 v236, s[8:11], s60 offen offset:0 lds\n\t"
    "buffer_load_dwordx4 v237, s[8:11], s60 offen offset:1024 lds\n\t"
    "v_lshl_add_u32 v240, s45, 7, v27\n\t"
    "v_add_u32 v241, 7168, v240\n\t"
    "v_add_u32 v242, 7168, v241\n\t"
    "v_add_u32 v243, 7168, v242\n\t"
    "s_mov_b32 s16, 0x7ff00000\n\t"
    "buffer_load_dwordx4 v238, s[8:11], s60 offen offset:2048 lds\n\t"
    "buffer_load_dwordx4 v239, s[8:11], s60 offen offset:3072 lds\n\t"
    "s_waitcnt vmcnt(16)\n\t"
    "s_barrier\n\t"
    "v_add_u32 v96, s96, v96\n\t"
    "v_add_u32 v97, s96, v97\n\t"
    "v_add_u32 v98, s96, v98\n\t"
    "v_add_u32 v99, s96, v99\n\t"
    "ds_read_b64_tr_b16 v[32:33], v98 offset:16384\n\t"
    "ds_read_b64_tr_b16 v[34:35], v98 offset:17408\n\t"
    "ds_read_b128 v[100:103], v16 offset:0\n\t"
    "s_add_u32 s60, s61, 192\n\t"
    "s_and_b32 s60, s60, 511\n\t"
    "s_mul_i32 s60, s60, 3584\n\t"
    "s_add_u32 m0, s69, 16384\n\t"
    "s_add_u32 s60, s60, s48\n\t"
    "buffer_load_dwordx4 v236, s[8:11], s60 offen offset:0 lds\n\t"
    "buffer_load_dwordx4 v237, s[8:11], s60 offen offset:1024 lds\n\t"
    "ds_read_b64_tr_b16 v[36:37], v98 offset:20480\n\t"
    "ds_read_b64_tr_b16 v[38:39], v98 offset:21504\n\t"
    "ds_read_b128 v[104:107], v17 offset:0\n\t"
    "buffer_load_dwordx4 v238, s[8:11], s60 offen offset:2048 lds\n\t"
    "buffer_load_dwordx4 v239, s[8:11], s60 offen offset:3072 lds\n\t"
    "ds_read_b64_tr_b16 v[40:41], v99 offset:16384\n\t"
    "ds_read_b64_tr_b16 v[42:43], v99 offset:17408\n\t"
    "ds_read_b128 v[108:111], v18 offset:0\n\t"
    "ds_read_b64_tr_b16 v[44:45], v99 offset:20480\n\t"
    "ds_read_b64_tr_b16 v[46:47], v99 offset:21504\n\t"
    "ds_read_b128 v[112:115], v19 offset:0\n\t"
    "s_add_u32 s60, s61, 208\n\t"
    "s_and_b32 s60, s60, 511\n\t"
    "s_mul_i32 s60, s60, 3584\n\t"
    "s_add_u32 m0, s69, 20480\n\t"
    "s_add_u32 s60, s60, s48\n\t"
    "buffer_load_dwordx4 v236, s[8:11], s60 offen offset:0 lds\n\t"
    "buffer_load_dwordx4 v237, s[8:11], s60 offen offset:1024 lds\n\t"
    "buffer_load_dwordx4 v238, s[8:11], s60 offen offset:2048 lds\n\t"
    "buffer_load_dwordx4 v239, s[8:11], s60 offen offset:3072 lds\n\t"
    "s_waitcnt vmcnt(16)\n\t"
    "s_barrier\n\t"
    "s_waitcnt lgkmcnt(6)\n\t"
    ".p2align 12\n\t"
    ".Lfmm_page3:\n\t"
    "s_cbranch_execz .Lfmm_ret\n\t"
    "v_mfma_f32_32x32x16_bf16 v[80:95], v[32:35], v[100:103], v[80:95]\n\t"
    "ds_read_b64_tr_b16 v[48:49], v98 offset:32768\n\t"
    "ds_read_b64_tr_b16 v[50:51], v98 offset:33792\n\t"
    "ds_read_b128 v[116:119], v16 offset:8192\n\t"
    "s_add_u32 s60, s61, 256\n\t"
    "s_and_b32 s60, s60, 511\n\t"
    "s_mul_i32 s60, s60, 3584\n\t"
    "s_add_u32 m0, s69, 98304\n\t"
    "s_add_u32 s60, s60, s48\n\t"
    "buffer_load_dwordx4 v236, s[8:11], s60 offen offset:0 lds\n\t"
    "buffer_load_dwordx4 v237, s[8:11], s60 offen offset:1024 lds\n\t"
    "v_mfma_f32_32x32x16_bf16 v[80:95], v[36:39], v[104:107], v[80:95]\n\t"
    "ds_read_b64_tr_b16 v[52:53], v98 offset:36864\n\t"
    "ds_read_b64_tr_b16 v[54:55], v98 offset:37888\n\t"
    "ds_read_b128 v[120:123], v17 offset:8192\n\t"
    "buffer_load_dwordx4 v238, s[8:11], s60 offen offset:2048 lds\n\t"
    "buffer_load_dwordx4 v239, s[8:11], s60 offen offset:3072 lds\n\t"
    "s_waitcnt lgkmcnt(6)\n\t"
    "v_mfma_f32_32x32x16_bf16 v[80:95], v[40:43], v[108:111], v[80:95]\n\t"
    "ds_read_b64_tr_b16 v[56:57], v99 offset:32768\n\t"
    "ds_read_b64_tr_b16 v[58:59], v99 offset:33792\n\t"
    "ds_read_b128 v[124:127], v18 offset:8192\n\t"
    "ds_read_b64_tr_b16 v[60:61], v99 offset:36864\n\t"
    "ds_read_b64_tr_b16 v[62:63], v99 offset:37888\n\t"
    "ds_read_b128 v[128:131], v19 offset:8192\n\t"
    "s_add_u32 s60, s61, 272\n\t"
    "s_and_b32 s60, s60, 511\n\t"
    "s_mul_i32 s60, s60, 3584\n\t"
    "s_add_u32 m0, s69, 102400\n\t"
    "s_add_u32 s60, s60, s48\n\t"
    "buffer_load_dwordx4 v236, s[8:11], s60 offen offset:0 lds\n\t"
    "buffer_load_dwordx4 v237, s[8:11], s60 offen offset:1024 lds\n\t"
    "v_mfma_f32_32x32x16_bf16 v[80:95], v[44:47], v[112:115], v[80:95]\n\t"
    "buffer_load_dwordx4 v238, s[8:11], s60 offen offset:2048 lds\n\t"
    "buffer_load_dwordx4 v239, s[8:11], s60 offen offset:3072 lds\n\t"
    "s_waitcnt vmcnt(16)\n\t"
    "s_barrier\n\t"
    "s_waitcnt lgkmcnt(6)\n\t"
    "v_mfma_f32_32x32x16_bf16 v[80:95], v[48:51], v[116:119], v[80:95]\n\t"
    "ds_read_b64_tr_b16 v[32:33], v96 offset:0\n\t"
    "ds_read_b64_tr_b16 v[34:35], v96 offset:1024\n\t"
    "ds_read_b128 v[132:135], v16 offset:16384\n\t"
    "s_add_u32 s60, s61, 320\n\t"
    "s_and_b32 s60, s60, 511\n\t"
    "s_mul_i32 s60, s60, 3584\n\t"
    "s_add_u32 m0, s69, 114688\n\t"
    "s_add_u32 s60, s60, s48\n\t"
    "buffer_load_dwordx4 v236, s[8:11], s60 offen offset:0 lds\n\t"
    "buffer_load_dwordx4 v237, s[8:11], s60 offen offset:1024 lds\n\t"
    "v_mfma_f32_32x32x16_bf16 v[80:95], v[52:55], v[120:123], v[80:95]\n\t"
    "ds_read_b64_tr_b16 v[36:37], v96 offset:4096\n\t"
    "ds_read_b64_tr_b16 v[38:39], v96 offset:5120\n\t"
    "ds_read_b128 v[136:139], v17 offset:16384\n\t"
    "buffer_load_dwordx4 v238, s[8:11], s60 offen offset:2048 lds\n\t"
    "buffer_load_dwordx4 v239, s[8:11], s60 offen offset:3072 lds\n\t"
    "s_waitcnt lgkmcnt(6)\n\t"
    "v_mfma_f32_32x32x16_bf16 v[80:95], v[56:59], v[124:127], v[80:95]\n\t"
    "ds_read_b64_tr_b16 v[40:41], v97 offset:0\n\t"
    "ds_read_b64_tr_b16 v[42:43], v97 offset:1024\n\t"
    "ds_read_b128 v[140:143], v18 offset:16384\n\t"
    "ds_read_b64_tr_b16 v[44:45], v97 offset:4096\n\t"
    "ds_read_b64_tr_b16 v[46:47], v97 offset:5120\n\t"
    "ds_read_b128 v[144:147], v19 offset:16384\n\t"
    "s_add_u32 s60, s61, 336\n\t"
    "s_and_b32 s60, s60, 511\n\t"
    "s_mul_i32 s60, s60, 3584\n\t"
    "s_add_u32 m0, s69, 118784\n\t"
    "s_add_u32 s60, s60, s48\n\t"
    "buffer_load_dwordx4 v236, s[8:11], s60 offen offset:0 lds\n\t"
    "buffer_load_dwordx4 v237, s[8:11], s60 offen offset:1024 lds\n\t"
    "v_mfma_f32_32x32x16_bf16 v[80:95], v[60:63], v[128:131], v[80:95]\n\t"
    "buffer_load_dwordx4 v238, s[8:11], s60 offen offset:2048 lds\n\t"
    "buffer_load_dwordx4 v239, s[8:11], s60 offen offset:3072 lds\n\t"
    "s_waitcnt vmcnt(16)\n\t"
    "s_barrier\n\t"
    "s_waitcnt lgkmcnt(6)\n\t"
    "v_mfma_f32_32x32x16_bf16 v[80:95], v[32:35], v[132:135], v[80:95]\n\t"
    "ds_read_b64_tr_b16 v[48:49], v96 offset:16384\n\t"
    "ds_read_b64_tr_b16 v[50:51], v96 offset:17408\n\t"
    "ds_read_b128 v[148:151], v16 offset:24576\n\t"
    "s_add_u32 s60, s61, 384\n\t"
    "s_and_b32 s60, s60, 511\n\t"
    "s_mul_i32 s60, s60, 3584\n\t"
    "s_add_u32 m0, s69, 131072\n\t"
    "s_add_u32 s60, s60, s48\n\t"
    "buffer_load_dwordx4 v236, s[8:11], s60 offen offset:0 lds\n\t"
    "buffer_load_dwordx4 v237, s[8:11], s60 offen offset:1024 lds\n\t"
    "v_mfma_f32_32x32x16_bf16 v[80:95], v[36:39], v[136:139], v[80:95]\n\t"
    "ds_read_b64_tr_b16 v[52:53], v96 offset:20480\n\t"
    "ds_read_b64_tr_b16 v[54:55], v96 offset:21504\n\t"
    "ds_read_b128 v[152:155], v17 offset:24576\n\t"
    "buffer_load_dwordx4 v238, s[8:11], s60 offen offset:2048 lds\n\t"
    "buffer_load_dwordx4 v239, s[8:11], s60 offen offset:3072 lds\n\t"
    "s_waitcnt lgkmcnt(6)\n\t"
    "v_mfma_f32_32x32x16_bf16 v[80:95], v[40:43], v[140:143], v[80:95]\n\t"
    "ds_read_b64_tr_b16 v[56:57], v97 offset:16384\n\t"
    "ds_read_b64_tr_b16 v[58:59], v97 offset:17408\n\t"
    "ds_read_b128 v[156:159], v18 offset:24576\n\t"
    "ds_read_b64_tr_b16 v[60:61], v97 offset:20480\n\t"
    "ds_read_b64_tr_b16 v[62:63], v97 offset:21504\n\t"
    "ds_read_b128 v[160:163], v19 offset:24576\n\t"
    "s_add_u32 s60, s61, 400\n\t"
    "s_and_b32 s60, s60, 511\n\t"
    "s_mul_i32 s60, s60, 3584\n\t"
    "s_add_u32 m0, s69, 135168\n\t"
    "s_add_u32 s60, s60, s48\n\t"
    "buffer_load_dwordx4 v236, s[8:11], s60 offen offset:0 lds\n\t"
    "buffer_load_dwordx4 v237, s[8:11], s60 offen offset:1024 lds\n\t"
    "v_mfma_f32_32x32x16_bf16 v[80:95], v[44:47], v[144:147], v[80:95]\n\t"
    "buffer_load_dwordx4 v238, s[8:11], s60 offen offset:2048 lds\n\t"
    "buffer_load_dwordx4 v239, s[8:11], s60 offen offset:3072 lds\n\t"
    "s_waitcnt vmcnt(16)\n\t"
    "s_barrier\n\t"
    "s_waitcnt lgkmcnt(6)\n\t"
    "v_mfma_f32_32x32x16_bf16 v[80:95], v[48:51], v[148:151], v[80:95]\n\t"
    "ds_read_b64_tr_b16 v[32:33], v98 offset:0\n\t"
    "ds_read_b64_tr_b16 v[34:35], v98 offset:1024\n\t"
    "ds_read_b128 v[164:167], v16 offset:32768\n\t"
    "s_add_u32 s60, s61, 448\n\t"
    "s_and_b32 s60, s60, 511\n\t"
    "s_mul_i32 s60, s60, 3584\n\t"
    "s_add_u32 m0, s69, 0\n\t"
    "s_add_u32 s60, s60, s48\n\t"
    "buffer_load_dwordx4 v236, s[8:11], s60 offen offset:0 lds\n\t"
    "buffer_load_dwordx4 v237, s[8:11], s60 offen offset:1024 lds\n\t"
    "v_mfma_f32_32x32x16_bf16 v[80:95], v[52:55], v[152:155], v[80:95]\n\t"
    "ds_read_b64_tr_b16 v[36:37], v98 offset:4096\n\t"
    "ds_read_b64_tr_b16 v[38:39], v98 offset:5120\n\t"
    "ds_read_b128 v[168:171], v17 offset:32768\n\t"
    "buffer_load_dwordx4 v238, s[8:11], s60 offen offset:2048 lds\n\t"
    "buffer_load_dwordx4 v239, s[8:11], s60 offen offset:3072 lds\n\t"
    "s_waitcnt lgkmcnt(6)\n\t"
    "v_mfma_f32_32x32x16_bf16 v[80:95], v[56:59], v[156:159], v[80:95]\n\t"
    "ds_read_b64_tr_b16 v[40:41], v99 offset:0\n\t"
    "ds_read_b64_tr_b16 v[42:43], v99 offset:1024\n\t"
    "ds_read_b128 v[172:175], v18 offset:32768\n\t"
    "ds_read_b64_tr_b16 v[44:45], v99 offset:4096\n\t"
    "ds_read_b64_tr_b16 v[46:47], v99 offset:5120\n\t"
    "ds_read_b128 v[176:179], v19 offset:32768\n\t"
    "s_add_u32 s60, s61, 464\n\t"
    "s_and_b32 s60, s60, 511\n\t"
    "s_mul_i32 s60, s60, 3584\n\t"
    "s_add_u32 m0, s69, 4096\n\t"
    "s_add_u32 s60, s60, s48\n\t"
    "buffer_load_dwordx4 v236, s[8:11], s60 offen offset:0 lds\n\t"
    "buffer_load_dwordx4 v237, s[8:11], s60 offen offset:1024 lds\n\t"
    "v_mfma_f32_32x32x16_bf16 v[80:95], v[60:63], v[160:163], v[80:95]\n\t"
    "buffer_load_dwordx4 v238, s[8:11], s60 offen offset:2048 lds\n\t"
    "buffer_load_dwordx4 v239, s[8:11], s60 offen offset:3072 lds\n\t"
    "s_waitcnt vmcnt(16)\n\t"
    "s_barrier\n\t"
    "s_waitcnt lgkmcnt(6)\n\t"
    "v_mfma_f32_32x32x16_bf16 v[80:95], v[32:35], v[164:167], v[80:95]\n\t"
    "ds_read_b64_tr_b16 v[48:49], v98 offset:16384\n\t"
    "ds_read_b64_tr_b16 v[50:51], v98 offset:17408\n\t"
    "ds_read_b128 v[180:183], v16 offset:40960\n\t"
    "s_add_u32 s60, s15, 0\n\t"
    "s_and_b32 s60, s60, 511\n\t"
    "s_add_u32 m0, s69, 16384\n\t"
    "s_lshl_b32 s60, s60, 10\n\t"
    "buffer_load_dwordx4 v240, s[28:31], s60 offen offset:0 lds\n\t"
    "buffer_load_dwordx4 v241, s[28:31], s60 offen offset:1024 lds\n\t"
    "v_mov_b32 v108, v11\n\t"
    "v_mov_b32 v109, v11\n\t"
    "v_mfma_f32_32x32x16_bf16 v[80:95], v[36:39], v[168:171], v[80:95]\n\t"
    "ds_read_b64_tr_b16 v[52:53], v98 offset:20480\n\t"
    "ds_read_b64_tr_b16 v[54:55], v98 offset:21504\n\t"
    "ds_read_b128 v[184:187], v17 offset:40960\n\t"
    "buffer_load_dwordx4 v242, s[28:31], s60 offen offset:2048 lds\n\t"
    "buffer_load_dwordx4 v243, s[28:31], s60 offen offset:3072 lds\n\t"
    "v_mov_b32 v110, v11\n\t"
    "s_waitcnt lgkmcnt(6)\n\t"
    "v_mfma_f32_32x32x16_bf16 v[80:95], v[40:43], v[172:175], v[80:95]\n\t"
    "ds_read_b64_tr_b16 v[56:57], v99 offset:16384\n\t"
    "ds_read_b64_tr_b16 v[58:59], v99 offset:17408\n\t"
    "ds_read_b128 v[188:191], v18 offset:40960\n\t"
    "ds_read_b64_tr_b16 v[60:61], v99 offset:20480\n\t"
    "ds_read_b64_tr_b16 v[62:63], v99 offset:21504\n\t"
    "ds_read_b128 v[192:195], v19 offset:40960\n\t"
    "s_add_u32 s60, s15, 32\n\t"
    "s_and_b32 s60, s60, 511\n\t"
    "s_add_u32 m0, s69, 20480\n\t"
    "s_lshl_b32 s60, s60, 10\n\t"
    "buffer_load_dwordx4 v240, s[28:31], s60 offen offset:0 lds\n\t"
    "buffer_load_dwordx4 v241, s[28:31], s60 offen offset:1024 lds\n\t"
    "v_mov_b32 v111, v11\n\t"
    "v_mfma_f32_32x32x16_bf16 v[80:95], v[44:47], v[176:179], v[80:95]\n\t"
    "buffer_load_dwordx4 v242, s[28:31], s60 offen offset:2048 lds\n\t"
    "buffer_load_dwordx4 v243, s[28:31], s60 offen offset:3072 lds\n\t"
    "s_waitcnt vmcnt(16)\n\t"
    "s_barrier\n\t"
    "s_waitcnt lgkmcnt(6)\n\t"
    "v_mfma_f32_32x32x16_bf16 v[80:95], v[48:51], v[180:183], v[80:95]\n\t"
    "ds_read_b64_tr_b16 v[32:33], v98 offset:32768\n\t"
    "ds_read_b64_tr_b16 v[34:35], v98 offset:33792\n\t"
    "ds_read_b128 v[196:199], v16 offset:49152\n\t"
    "s_add_u32 s60, s15, 128\n\t"
    "s_and_b32 s60, s60, 511\n\t"
    "s_add_u32 m0, s69, 98304\n\t"
    "s_lshl_b32 s60, s60, 10\n\t"
    "buffer_load_dwordx4 v240, s[28:31], s60 offen offset:0 lds\n\t"
    "buffer_load_dwordx4 v241, s[28:31], s60 offen offset:1024 lds\n\t"
    "v_mov_b32 v112, v11\n\t"
    "v_mov_b32 v113, v11\n\t"
    "v_mfma_f32_32x32x16_bf16 v[80:95], v[52:55], v[184:187], v[80:95]\n\t"
    "ds_read_b64_tr_b16 v[36:37], v98 offset:36864\n\t"
    "ds_read_b64_tr_b16 v[38:39], v98 offset:37888\n\t"
    "ds_read_b128 v[200:203], v17 offset:49152\n\t"
    "buffer_load_dwordx4 v242, s[28:31], s60 offen offset:2048 lds\n\t"
    "buffer_load_dwordx4 v243, s[28:31], s60 offen offset:3072 lds\n\t"
    "v_mov_b32 v114, v11\n\t"
    "s_waitcnt lgkmcnt(6)\n\t"
    "v_mfma_f32_32x32x16_bf16 v[80:95], v[56:59], v[188:191], v[80:95]\n\t"
    "ds_read_b64_tr_b16 v[40:41], v99 offset:32768\n\t"
    "ds_read_b64_tr_b16 v[42:43], v99 offset:33792\n\t"
    "ds_read_b128 v[204:207], v18 offset:49152\n\t"
    "ds_read_b64_tr_b16 v[44:45], v99 offset:36864\n\t"
    "ds_read_b64_tr_b16 v[46:47], v99 offset:37888\n\t"
    "ds_read_b128 v[208:211], v19 offset:49152\n\t"
    "s_add_u32 s60, s15, 160\n\t"
    "s_and_b32 s60, s60, 511\n\t"
    "s_add_u32 m0, s69, 102400\n\t"
    "s_lshl_b32 s60, s60, 10\n\t"
    "buffer_load_dwordx4 v240, s[28:31], s60 offen offset:0 lds\n\t"
    "buffer_load_dwordx4 v241, s[28:31], s60 offen offset:1024 lds\n\t"
    "v_mov_b32 v115, v11\n\t"
    "v_mfma_f32_32x32x16_bf16 v[80:95], v[60:63], v[192:195], v[80:95]\n\t"
    "buffer_load_dwordx4 v242, s[28:31], s60 offen offset:2048 lds\n\t"
    "buffer_load_dwordx4 v243, s[28:31], s60 offen offset:3072 lds\n\t"
    "s_waitcnt vmcnt(16)\n\t"
    "s_barrier\n\t"
    "s_waitcnt lgkmcnt(6)\n\t"
    "v_mfma_f32_32x32x16_bf16 v[80:95], v[32:35], v[196:199], v[80:95]\n\t"
    "ds_read_b64_tr_b16 v[48:49], v96 offset:0\n\t"
    "ds_read_b64_tr_b16 v[50:51], v96 offset:1024\n\t"
    "ds_read_b128 v[212:215], v16 offset:57344\n\t"
    "s_add_u32 s60, s15, 256\n\t"
    "s_and_b32 s60, s60, 511\n\t"
    "s_add_u32 m0, s69, 114688\n\t"
    "s_lshl_b32 s60, s60, 10\n\t"
    "buffer_load_dwordx4 v240, s[28:31], s60 offen offset:0 lds\n\t"
    "buffer_load_dwordx4 v241, s[28:31], s60 offen offset:1024 lds\n\t"
    "v_mov_b32 v15, 0xdddddddd\n\t"
    "v_mfma_f32_32x32x16_bf16 v[80:95], v[36:39], v[200:203], v[80:95]\n\t"
    "ds_read_b64_tr_b16 v[52:53], v96 offset:4096\n\t"
    "ds_read_b64_tr_b16 v[54:55], v96 offset:5120\n\t"
    "ds_read_b128 v[216:219], v17 offset:57344\n\t"
    "buffer_load_dwordx4 v242, s[28:31], s60 offen offset:2048 lds\n\t"
    "buffer_load_dwordx4 v243, s[28:31], s60 offen offset:3072 lds\n\t"
    "s_mov_b32 s66, 0xffff\n\t"
    "s_waitcnt lgkmcnt(6)\n\t"
    "v_mfma_f32_32x32x16_bf16 v[80:95], v[40:43], v[204:207], v[80:95]\n\t"
    "ds_read_b64_tr_b16 v[56:57], v97 offset:0\n\t"
    "ds_read_b64_tr_b16 v[58:59], v97 offset:1024\n\t"
    "ds_read_b128 v[220:223], v18 offset:57344\n\t"
    "ds_read_b64_tr_b16 v[60:61], v97 offset:4096\n\t"
    "ds_read_b64_tr_b16 v[62:63], v97 offset:5120\n\t"
    "ds_read_b128 v[224:227], v19 offset:57344\n\t"
    "s_add_u32 s60, s15, 288\n\t"
    "s_and_b32 s60, s60, 511\n\t"
    "s_add_u32 m0, s69, 118784\n\t"
    "s_lshl_b32 s60, s60, 10\n\t"
    "buffer_load_dwordx4 v240, s[28:31], s60 offen offset:0 lds\n\t"
    "buffer_load_dwordx4 v241, s[28:31], s60 offen offset:1024 lds\n\t"
    "s_mov_b32 s67, 0xffff\n\t"
    "v_mfma_f32_32x32x16_bf16 v[80:95], v[44:47], v[208:211], v[80:95]\n\t"
    "buffer_load_dwordx4 v242, s[28:31], s60 offen offset:2048 lds\n\t"
    "buffer_load_dwordx4 v243, s[28:31], s60 offen offset:3072 lds\n\t"
    "s_waitcnt vmcnt(16)\n\t"
    "s_barrier\n\t"
    "s_waitcnt lgkmcnt(6)\n\t"
    "v_mfma_f32_32x32x16_bf16 v[80:95], v[48:51], v[212:215], v[80:95]\n\t"
    "ds_read_b64_tr_b16 v[32:33], v254 offset:16384\n\t"
    "ds_read_b64_tr_b16 v[34:35], v254 offset:16640\n\t"
    "s_add_u32 s60, s15, 384\n\t"
    "s_and_b32 s60, s60, 511\n\t"
    "s_add_u32 m0, s69, 131072\n\t"
    "s_lshl_b32 s60, s60, 10\n\t"
    "buffer_load_dwordx4 v240, s[28:31], s60 offen offset:0 lds\n\t"
    "buffer_load_dwordx4 v241, s[28:31], s60 offen offset:1024 lds\n\t"
    "v_mfma_f32_32x32x16_bf16 v[80:95], v[52:55], v[216:219], v[80:95]\n\t"
    "ds_read_b64_tr_b16 v[36:37], v254 offset:18432\n\t"
    "ds_read_b64_tr_b16 v[38:39], v254 offset:18688\n\t"
    "buffer_load_dwordx4 v242, s[28:31], s60 offen offset:2048 lds\n\t"
    "buffer_load_dwordx4 v243, s[28:31], s60 offen offset:3072 lds\n\t"
    "s_waitcnt lgkmcnt(4)\n\t"
    "v_mfma_f32_32x32x16_bf16 v[80:95], v[56:59], v[220:223], v[80:95]\n\t"
    "ds_read_b64_tr_b16 v[40:41], v254 offset:24576\n\t"
    "ds_read_b64_tr_b16 v[42:43], v254 offset:24832\n\t"
    "ds_read_b64_tr_b16 v[44:45], v254 offset:26624\n\t"
    "ds_read_b64_tr_b16 v[46:47], v254 offset:26880\n\t"
    "s_add_u32 s60, s15, 416\n\t"
    "s_and_b32 s60, s60, 511\n\t"
    "s_add_u32 m0, s69, 135168\n\t"
    "s_lshl_b32 s60, s60, 10\n\t"
    "buffer_load_dwordx4 v240, s[28:31], s60 offen offset:0 lds\n\t"
    "buffer_load_dwordx4 v241, s[28:31], s60 offen offset:1024 lds\n\t"
    "v_mfma_f32_32x32x16_bf16 v[80:95], v[60:63], v[224:227], v[80:95]\n\t"
    "buffer_load_dwordx4 v242, s[28:31], s60 offen offset:2048 lds\n\t"
    "buffer_load_dwordx4 v243, s[28:31], s60 offen offset:3072 lds\n\t"
    "s_waitcnt vmcnt(16)\n\t"
    "s_barrier\n\t"
    "s_sub_u32 s62, s54, 3\n\t"
    "s_waitcnt lgkmcnt(4)\n\t"
    "v_mfma_f32_32x32x16_bf16 v[64:79], v[32:35], v[100:103], 0\n\t"
    "ds_read_b64_tr_b16 v[48:49], v255 offset:0\n\t"
    "ds_read_b64_tr_b16 v[50:51], v255 offset:256\n\t"
    "s_add_u32 s60, s62, 0\n\t"
    "s_and_b32 s60, s60, 63\n\t"
    "s_add_u32 m0, s69, 32768\n\t"
    "s_lshl_b32 s60, s60, 11\n\t"
    "buffer_load_dwordx4 v4, s[20:23], s60 offen offset:0 sc1 lds\n\t"
    "buffer_load_dwordx4 v4, s[20:23], s60 offen offset:1024 sc1 lds\n\t"
    "v_mfma_f32_32x32x16_bf16 v[64:79], v[36:39], v[104:107], v[64:79]\n\t"
    "ds_read_b64_tr_b16 v[52:53], v255 offset:2048\n\t"
    "ds_read_b64_tr_b16 v[54:55], v255 offset:2304\n\t"
    "buffer_load_dwordx4 v4, s[20:23], s60 offen offset:2048 sc1 lds\n\t"
    "buffer_load_dwordx4 v4, s[20:23], s60 offen offset:3072 sc1 lds\n\t"
    "v_exp_f32 v228, v80\n\t"
    "v_exp_f32 v229, v81\n\t"
    "v_exp_f32 v230, v82\n\t"
    "v_exp_f32 v231, v83\n\t"
    "s_waitcnt lgkmcnt(4)\n\t"
    "v_mfma_f32_32x32x16_bf16 v[64:79], v[40:43], v[116:119], v[64:79]\n\t"
    "ds_read_b64_tr_b16 v[56:57], v255 offset:8192\n\t"
    "ds_read_b64_tr_b16 v[58:59], v255 offset:8448\n\t"
    "ds_read_b64_tr_b16 v[60:61], v255 offset:10240\n\t"
    "ds_read_b64_tr_b16 v[62:63], v255 offset:10496\n\t"
    "s_add_u32 s60, s62, 2\n\t"
    "s_and_b32 s60, s60, 63\n\t"
    "s_add_u32 m0, s69, 36864\n\t"
    "s_lshl_b32 s60, s60, 11\n\t"
    "buffer_load_dwordx4 v4, s[20:23], s60 offen offset:0 sc1 lds\n\t"
    "buffer_load_dwordx4 v4, s[20:23], s60 offen offset:1024 sc1 lds\n\t"
    "v_exp_f32 v232, v84\n\t"
    "v_exp_f32 v233, v85\n\t"
    "v_exp_f32 v234, v86\n\t"
    "v_exp_f32 v235, v87\n\t"
    "v_fma_f32 v228, v228, v228, 1.0\n\t"
    "v_fma_f32 v229, v229, v229, 1.0\n\t"
    "v_mfma_f32_32x32x16_bf16 v[64:79], v[44:47], v[120:123], v[64:79]\n\t"
    "buffer_load_dwordx4 v4, s[20:23], s60 offen offset:2048 sc1 lds\n\t"
    "buffer_load_dwordx4 v4, s[20:23], s60 offen offset:3072 sc1 lds\n\t"
    "s_waitcnt vmcnt(16)\n\t"
    "s_barrier\n\t"
    "s_waitcnt lgkmcnt(4)\n\t"
    "v_mfma_f32_32x32x16_bf16 v[64:79], v[48:51], v[132:135], v[64:79]\n\t"
    "ds_read_b64_tr_b16 v[32:33], v255 offset:16384\n\t"
    "ds_read_b64_tr_b16 v[34:35], v255 offset:16640\n\t"
    "s_add_u32 s60, s62, 8\n\t"
    "s_and_b32 s60, s60, 63\n\t"
    "s_add_u32 m0, s69, 49152\n\t"
    "s_lshl_b32 s60, s60, 11\n\t"
    "buffer_load_dwordx4 v4, s[20:23], s60 offen offset:0 sc1 lds\n\t"
    "buffer_load_dwordx4 v4, s[20:23], s60 offen offset:1024 sc1 lds\n\t"
    "v_fma_f32 v230, v230, v230, 1.0\n\t"
    "v_fma_f32 v231, v231, v231, 1.0\n\t"
    "v_fma_f32 v232, v232, v232, 1.0\n\t"
    "v_fma_f32 v233, v233, v233, 1.0\n\t"
    "v_fma_f32 v234, v234, v234, 1.0\n\t"
    "v_mfma_f32_32x32x16_bf16 v[64:79], v[52:55], v[136:139], v[64:79]\n\t"
    "ds_read_b64_tr_b16 v[36:37], v255 offset:18432\n\t"
    "ds_read_b64_tr_b16 v[38:39], v255 offset:18688\n\t"
    "buffer_load_dwordx4 v4, s[20:23], s60 offen offset:2048 sc1 lds\n\t"
    "buffer_load_dwordx4 v4, s[20:23], s60 offen offset:3072 sc1 lds\n\t"
    "v_mov_b32 v104, 0\n\t"
    "v_mov_b32 v105, 0\n\t"
    "v_fma_f32 v235, v235, v235, 1.0\n\t"
    "v_fma_f32 v228, v228, v228, 1.0\n\t"
    "v_fma_f32 v229, v229, v229, 1.0\n\t"
    "v_fma_f32 v230, v230, v230, 1.0\n\t"
    "s_waitcnt lgkmcnt(4)\n\t"
    "v_mfma_f32_32x32x16_bf16 v[64:79], v[56:59], v[148:151], v[64:79]\n\t"
    "ds_read_b64_tr_b16 v[40:41], v255 offset:24576\n\t"
    "ds_read_b64_tr_b16 v[42:43], v255 offset:24832\n\t"
    "ds_read_b64_tr_b16 v[44:45], v255 offset:26624\n\t"
    "ds_read_b64_tr_b16 v[46:47], v255 offset:26880\n\t"
    "s_add_u32 s60, s62, 10\n\t"
    "s_and_b32 s60, s60, 63\n\t"
    "s_add_u32 m0, s69, 53248\n\t"
    "s_lshl_b32 s60, s60, 11\n\t"
    "buffer_load_dwordx4 v4, s[20:23], s60 offen offset:0 sc1 lds\n\t"
    "buffer_load_dwordx4 v4, s[20:23], s60 offen offset:1024 sc1 lds\n\t"
    "v_fma_f32 v231, v231, v231, 1.0\n\t"
    "v_fma_f32 v232, v232, v232, 1.0\n\t"
    "v_fma_f32 v233, v233, v233, 1.0\n\t"
    "v_fma_f32 v234, v234, v234, 1.0\n\t"
    "v_fma_f32 v235, v235, v235, 1.0\n\t"
    "v_mfma_f32_32x32x16_bf16 v[64:79], v[60:63], v[152:155], v[64:79]\n\t"
    "buffer_load_dwordx4 v4, s[20:23], s60 offen offset:2048 sc1 lds\n\t"
    "buffer_load_dwordx4 v4, s[20:23], s60 offen offset:3072 sc1 lds\n\t"
    "s_waitcnt vmcnt(16)\n\t"
    "s_barrier\n\t"
    "s_waitcnt lgkmcnt(4)\n\t"
    "v_mfma_f32_32x32x16_bf16 v[64:79], v[32:35], v[164:167], v[64:79]\n\t"
    "ds_read_b64_tr_b16 v[48:49], v255 offset:32768\n\t"
    "ds_read_b64_tr_b16 v[50:51], v255 offset:33024\n\t"
    "s_add_u32 s60, s62, 16\n\t"
    "s_and_b32 s60, s60, 63\n\t"
    "s_add_u32 m0, s69, 65536\n\t"
    "s_lshl_b32 s60, s60, 11\n\t"
    "buffer_load_dwordx4 v4, s[20:23], s60 offen offset:0 sc1 lds\n\t"
    "buffer_load_dwordx4 v4, s[20:23], s60 offen offset:1024 sc1 lds\n\t"
    "v_rcp_f32 v228, v228 mul:2\n\t"
    "v_rcp_f32 v229, v229 mul:2\n\t"
    "v_rcp_f32 v230, v230 mul:2\n\t"
    "v_rcp_f32 v231, v231 mul:2\n\t"
    "v_rcp_f32 v232, v232 mul:2\n\t"
    "v_rcp_f32 v233, v233 mul:2\n\t"
    "v_mfma_f32_32x32x16_bf16 v[64:79], v[36:39], v[168:171], v[64:79]\n\t"
    "ds_read_b64_tr_b16 v[52:53], v255 offset:34816\n\t"
    "ds_read_b64_tr_b16 v[54:55], v255 offset:35072\n\t"
    "buffer_load_dwordx4 v4, s[20:23], s60 offen offset:2048 sc1 lds\n\t"
    "buffer_load_dwordx4 v4, s[20:23], s60 offen offset:3072 sc1 lds\n\t"
    "v_mfma_f32_32x32x8_bf16 v[116:131], v[2:3], v[104:105], 0\n\t"
    "v_rcp_f32 v234, v234 mul:2\n\t"
    "v_rcp_f32 v235, v235 mul:2\n\t"
    "v_fma_f32 v228, -v80, v228, v80\n\t"
    "v_fma_f32 v229, -v81, v229, v81\n\t"
    "v_fma_f32 v230, -v82, v230, v82\n\t"
    "v_fma_f32 v231, -v83, v231, v83\n\t"
    "v_fma_f32 v232, -v84, v232, v84\n\t"
    "v_fma_f32 v233, -v85, v233, v85\n\t"
    "v_fma_f32 v234, -v86, v234, v86\n\t"
    "v_fma_f32 v235, -v87, v235, v87\n\t"
    "s_waitcnt lgkmcnt(4)\n\t"
    "v_mfma_f32_32x32x16_bf16 v[64:79], v[40:43], v[180:183], v[64:79]\n\t"
    "ds_read_b64_tr_b16 v[56:57], v255 offset:40960\n\t"
    "ds_read_b64_tr_b16 v[58:59], v255 offset:41216\n\t"
    "ds_read_b64_tr_b16 v[60:61], v255 offset:43008\n\t"
    "ds_read_b64_tr_b16 v[62:63], v255 offset:43264\n\t"
    "s_add_u32 s60, s62, 18\n\t"
    "s_and_b32 s60, s60, 63\n\t"
    "s_add_u32 m0, s69, 69632\n\t"
    "s_lshl_b32 s60, s60, 11\n\t"
    "buffer_load_dwordx4 v4, s[20:23], s60 offen offset:0 sc1 lds\n\t"
    "buffer_load_dwordx4 v4, s[20:23], s60 offen offset:1024 sc1 lds\n\t"
    "v_cvt_pk_bf16_f32 v244, v228, v229\n\t"
    "v_cvt_pk_bf16_f32 v245, v230, v231\n\t"
    "v_cvt_pk_bf16_f32 v246, v232, v233\n\t"
    "v_cvt_pk_bf16_f32 v247, v234, v235\n\t"
    "v_exp_f32 v228, v88\n\t"
    "v_exp_f32 v229, v89\n\t"
    "v_exp_f32 v230, v90\n\t"
    "v_exp_f32 v231, v91\n\t"
    "v_mfma_f32_32x32x16_bf16 v[64:79], v[44:47], v[184:187], v[64:79]\n\t"
    "buffer_load_dwordx4 v4, s[20:23], s60 offen offset:2048 sc1 lds\n\t"
    "buffer_load_dwordx4 v4, s[20:23], s60 offen offset:3072 sc1 lds\n\t"
    "s_waitcnt lgkmcnt(4)\n\t"
    "v_mfma_f32_32x32x16_bf16 v[64:79], v[48:51], v[196:199], v[64:79]\n\t"
    "v_exp_f32 v232, v92\n\t"
    "v_exp_f32 v233, v93\n\t"
    "v_exp_f32 v234, v94\n\t"
    "v_mfma_f32_32x32x16_bf16 v[64:79], v[52:55], v[200:203], v[64:79]\n\t"
    "v_exp_f32 v235, v95\n\t"
    "v_fma_f32 v228, v228, v228, 1.0\n\t"
    "v_fma_f32 v229, v229, v229, 1.0\n\t"
    "v_fma_f32 v230, v230, v230, 1.0\n\t"
    "v_fma_f32 v231, v231, v231, 1.0\n\t"
    "v_fma_f32 v232, v232, v232, 1.0\n\t"
    "s_waitcnt lgkmcnt(0)\n\t"
    "v_mfma_f32_32x32x16_bf16 v[64:79], v[56:59], v[212:215], v[64:79]\n\t"
    "v_fma_f32 v233, v233, v233, 1.0\n\t"
    "v_fma_f32 v234, v234, v234, 1.0\n\t"
    "v_fma_f32 v235, v235, v235, 1.0\n\t"
    "v_fma_f32 v228, v228, v228, 1.0\n\t"
    "v_fma_f32 v229, v229, v229, 1.0\n\t"
    "v_fma_f32 v230, v230, v230, 1.0\n\t"
    "v_fma_f32 v231, v231, v231, 1.0\n\t"
    "v_mfma_f32_32x32x16_bf16 v[64:79], v[60:63], v[216:219], v[64:79]\n\t"
    "s_xor_b32 s56, s44, 2\n\t"
    "s_mul_i32 s55, s44, 3072\n\t"
    "s_mul_i32 s56, s56, 3072\n\t"
    "v_fma_f32 v232, v232, v232, 1.0\n\t"
    "v_fma_f32 v233, v233, v233, 1.0\n\t"
    "v_fma_f32 v234, v234, v234, 1.0\n\t"
    "v_fma_f32 v235, v235, v235, 1.0\n\t"
    "v_rcp_f32 v228, v228 mul:2\n\t"
    "v_rcp_f32 v229, v229 mul:2\n\t"
    "v_rcp_f32 v230, v230 mul:2\n\t"
    "v_rcp_f32 v231, v231 mul:2\n\t"
    "v_rcp_f32 v232, v232 mul:2\n\t"
    "v_rcp_f32 v233, v233 mul:2\n\t"
    "ds_write_b128 v28, v[64:67] offset:0\n\t"
    "ds_write_b128 v28, v[68:71] offset:1024\n\t"
    "ds_write_b128 v28, v[72:75] offset:16384\n\t"
    "ds_write_b128 v28, v[76:79] offset:17408\n\t"
    "v_rcp_f32 v234, v234 mul:2\n\t"
    "v_rcp_f32 v235, v235 mul:2\n\t"
    "v_fma_f32 v228, -v88, v228, v88\n\t"
    "v_fma_f32 v229, -v89, v229, v89\n\t"
    "s_waitcnt vmcnt(8) lgkmcnt(0)\n\t"
    "s_barrier\n\t"
    "s_setprio 3\n\t"
    "ds_read_b128 v[48:51], v29 offset:0\n\t"
    "ds_read_b128 v[52:55], v29 offset:1024\n\t"
    "ds_read_b128 v[100:103], v14 offset:0\n\t"
    "ds_read_b128 v[56:59], v29 offset:16384\n\t"
    "ds_read_b128 v[60:63], v29 offset:17408\n\t"
    "s_mov_b64 exec, s[66:67]\n\t"
    "ds_read_b128 v[108:111], v5 offset:0\n\t"
    "ds_read_b128 v[112:115], v5 offset:512\n\t"
    "s_mov_b64 exec, -1\n\t"
    "v_mfma_f32_32x32x8_bf16 v[16:31], v[2:3], v[116:117], 0\n\t"
    "v_fma_f32 v230, -v90, v230, v90\n\t"
    "v_fma_f32 v231, -v91, v231, v91\n\t"
    "v_fma_f32 v232, -v92, v232, v92\n\t"
    "v_fma_f32 v233, -v93, v233, v93\n\t"
    "v_fma_f32 v234, -v94, v234, v94\n\t"
    "v_fma_f32 v235, -v95, v235, v95\n\t"
    "s_waitcnt lgkmcnt(5)\n\t"
    "v_pk_add_f32 v[72:73], v[72:73], v[48:49]\n\t"
    "v_pk_add_f32 v[74:75], v[74:75], v[50:51]\n\t"
    "v_pk_add_f32 v[76:77], v[76:77], v[52:53]\n\t"
    "v_pk_add_f32 v[78:79], v[78:79], v[54:55]\n\t"
    "v_cvt_pk_bf16_f32 v104, v72, v73\n\t"
    "v_cvt_pk_bf16_f32 v105, v74, v75\n\t"
    "v_cvt_pk_bf16_f32 v106, v76, v77\n\t"
    "v_cvt_pk_bf16_f32 v107, v78, v79\n\t"
    "s_waitcnt lgkmcnt(4)\n\t"
    "s_nop 0\n\t"
    "v_mfma_f32_32x32x16_bf16 v[32:47], v[100:103], v[104:107], -4.0\n\t"
    "s_waitcnt lgkmcnt(2)\n\t"
    "v_pk_add_f32 v[64:65], v[64:65], v[56:57]\n\t"
    "v_pk_add_f32 v[66:67], v[66:67], v[58:59]\n\t"
    "v_pk_add_f32 v[68:69], v[68:69], v[60:61]\n\t"
    "v_pk_add_f32 v[70:71], v[70:71], v[62:63]\n\t"
    "v_cvt_pk_bf16_f32 v96, v64, v65\n\t"
    "v_cvt_pk_bf16_f32 v97, v66, v67\n\t"
    "v_cvt_pk_bf16_f32 v98, v68, v69\n\t"
    "v_cvt_pk_bf16_f32 v99, v70, v71\n\t"
    "v_cvt_pk_bf16_f32 v248, v228, v229\n\t"
    "v_cvt_pk_bf16_f32 v249, v230, v231\n\t"
    "v_cvt_pk_bf16_f32 v250, v232, v233\n\t"
    "v_cvt_pk_bf16_f32 v251, v234, v235\n\t"
    "v_add_u32 v2, s55, v4\n\t"
    "v_add_u32 v3, s56, v4\n\t"
    "v_mov_b32 v132, -1\n\t"
    "v_mov_b32 v133, -1\n\t"
    "v_mov_b32 v134, -1\n\t"
    "v_mov_b32 v135, -1\n\t"
    "s_mul_i32 s60, s2, 7\n\t"
    "v_min_u32 v140, 0x60, v4\n\t"
    "v_and_b32 v141, 16, v4\n\t"
    "v_lshlrev_b32 v140, 3, v140\n\t"
    "v_lshlrev_b32 v141, 3, v141\n\t"
    "v_lshl_add_u32 v140, s60, 7, v140\n\t"
    "v_lshl_add_u32 v141, s2, 8, v141\n\t"
    ".Lfmm_headend:\n\t"
    "v_exp_f32 v32, v32\n\t"
    "v_exp_f32 v33, v33\n\t"
    "v_exp_f32 v34, v34\n\t"
    "s_add_u32 s1, s1, 0x4000\n\t"
    "v_exp_f32 v35, v35\n\t"
    "s_and_b32 s1, s1, 0x1ffff\n\t"
    "v_exp_f32 v36, v36\n\t"
    "s_add_u32 m0, s46, 81920\n\t"
    "v_exp_f32 v37, v37\n\t"
    "v_mfma_f32_32x32x16_bf16 v[48:63], v[100:103], v[96:99], -4.0\n\t"
    "ds_read_b128 v[100:103], v14 offset:4096\n\t"
    "buffer_load_dwordx4 v4, s[20:23], s1 offen sc1 lds\n\t"
    "v_exp_f32 v38, v38\n\t"
    "v_exp_f32 v39, v39\n\t"
    "v_exp_f32 v40, v40\n\t"
    "s_waitcnt lgkmcnt(2)\n\t"
    "v_exp_f32 v41, v41\n\t"
    "v_smfmac_f32_32x32x32_bf16 v[16:31], v[108:111], v[32:39], v15\n\t"
    "buffer_load_dwordx4 v4, s[20:23], s1 offen offset:1024 sc1 lds\n\t"
    "v_exp_f32 v42, v42\n\t"
    "v_exp_f32 v43, v43\n\t"
    "v_exp_f32 v44, v44\n\t"
    "v_exp_f32 v45, v45\n\t"
    "v_exp_f32 v46, v46\n\t"
    "v_exp_f32 v47, v47\n\t"
    "v_exp_f32 v48, v48\n\t"
    "s_waitcnt lgkmcnt(0)\n\t"
    "v_exp_f32 v49, v49\n\t"
    "v_smfmac_f32_32x32x32_bf16 v[16:31], v[112:115], v[40:47], v15\n\t"
    "buffer_store_dwordx4 v[244:247], v4, s[20:23], s17 offen offset:0 sc1\n\t"
    "v_exp_f32 v50, v50\n\t"
    "s_add_u32 s1, s1, 0x4000\n\t"
    "v_exp_f32 v51, v51\n\t"
    "s_and_b32 s1, s1, 0x1ffff\n\t"
    "v_exp_f32 v52, v52\n\t"
    "s_add_u32 m0, s46, 98304\n\t"
    "v_exp_f32 v53, v53\n\t"
    "v_mfma_f32_32x32x16_bf16 v[32:47], v[100:103], v[104:107], -4.0\n\t"
    "buffer_load_dwordx4 v4, s[20:23], s1 offen sc1 lds\n\t"
    ".p2align 12\n\t"
    ".Lfmm_page4:\n\t"
    "s_cbranch_execz .Lfmm_ret\n\t"
    "v_exp_f32 v54, v54\n\t"
    "v_exp_f32 v55, v55\n\t"
    "v_exp_f32 v56, v56\n\t"
    "v_exp_f32 v57, v57\n\t"
    "v_smfmac_f32_32x32x32_bf16 v[116:131], v[108:111], v[48:55], v15\n\t"
    "buffer_load_dwordx4 v4, s[20:23], s1 offen offset:1024 sc1 lds\n\t"
    "v_exp_f32 v58, v58\n\t"
    "v_exp_f32 v59, v59\n\t"
    "v_exp_f32 v60, v60\n\t"
    "v_exp_f32 v61, v61\n\t"
    "v_exp_f32 v62, v62\n\t"
    "v_exp_f32 v63, v63\n\t"
    "v_exp_f32 v32, v32\n\t"
    "v_exp_f32 v33, v33\n\t"
    "v_smfmac_f32_32x32x32_bf16 v[116:131], v[112:115], v[56:63], v15\n\t"
    "s_mov_b64 exec, s[66:67]\n\t"
    "ds_read_b128 v[108:111], v5 offset:4096\n\t"
    "ds_read_b128 v[112:115], v5 offset:4608\n\t"
    "s_mov_b64 exec, -1\n\t"
    "v_exp_f32 v34, v34\n\t"
    "s_add_u32 s1, s1, 0x4000\n\t"
    "v_exp_f32 v35, v35\n\t"
    "s_and_b32 s1, s1, 0x1ffff\n\t"
    "v_exp_f32 v36, v36\n\t"
    "s_add_u32 m0, s46, 114688\n\t"
    "v_exp_f32 v37, v37\n\t"
    "v_mfma_f32_32x32x16_bf16 v[48:63], v[100:103], v[96:99], -4.0\n\t"
    "ds_read_b128 v[100:103], v14 offset:8192\n\t"
    "buffer_load_dwordx4 v4, s[20:23], s1 offen sc1 lds\n\t"
    "v_exp_f32 v38, v38\n\t"
    "v_exp_f32 v39, v39\n\t"
    "v_exp_f32 v40, v40\n\t"
    "s_waitcnt lgkmcnt(2)\n\t"
    "v_exp_f32 v41, v41\n\t"
    "v_smfmac_f32_32x32x32_bf16 v[16:31], v[108:111], v[32:39], v15\n\t"
    "buffer_load_dwordx4 v4, s[20:23], s1 offen offset:1024 sc1 lds\n\t"
    "v_exp_f32 v42, v42\n\t"
    "v_exp_f32 v43, v43\n\t"
    "v_exp_f32 v44, v44\n\t"
    "v_exp_f32 v45, v45\n\t"
    "v_exp_f32 v46, v46\n\t"
    "v_exp_f32 v47, v47\n\t"
    "v_exp_f32 v48, v48\n\t"
    "s_waitcnt lgkmcnt(0)\n\t"
    "v_exp_f32 v49, v49\n\t"
    "v_smfmac_f32_32x32x32_bf16 v[16:31], v[112:115], v[40:47], v15\n\t"
    "buffer_store_dwordx4 v[248:251], v4, s[20:23], s17 offen offset:2048 sc1\n\t"
    "v_exp_f32 v50, v50\n\t"
    "s_add_u32 s1, s1, 0x4000\n\t"
    "v_exp_f32 v51, v51\n\t"
    "s_and_b32 s1, s1, 0x1ffff\n\t"
    "v_exp_f32 v52, v52\n\t"
    "s_add_u32 m0, s46, 0\n\t"
    "v_exp_f32 v53, v53\n\t"
    "v_mfma_f32_32x32x16_bf16 v[32:47], v[100:103], v[104:107], -4.0\n\t"
    "buffer_load_dwordx4 v4, s[20:23], s1 offen sc1 lds\n\t"
    "v_exp_f32 v54, v54\n\t"
    "v_exp_f32 v55, v55\n\t"
    "v_exp_f32 v56, v56\n\t"
    "v_exp_f32 v57, v57\n\t"
    "v_smfmac_f32_32x32x32_bf16 v[116:131], v[108:111], v[48:55], v15\n\t"
    "buffer_load_dwordx4 v4, s[20:23], s1 offen offset:1024 sc1 lds\n\t"
    "v_exp_f32 v58, v58\n\t"
    "v_exp_f32 v59, v59\n\t"
    "v_exp_f32 v60, v60\n\t"
    "v_exp_f32 v61, v61\n\t"
    "v_exp_f32 v62, v62\n\t"
    "v_exp_f32 v63, v63\n\t"
    "v_exp_f32 v32, v32\n\t"
    "v_exp_f32 v33, v33\n\t"
    "v_smfmac_f32_32x32x32_bf16 v[116:131], v[112:115], v[56:63], v15\n\t"
    "s_mov_b64 exec, s[66:67]\n\t"
    "ds_read_b128 v[108:111], v5 offset:8192\n\t"
    "ds_read_b128 v[112:115], v5 offset:8704\n\t"
    "s_mov_b64 exec, -1\n\t"
    "v_exp_f32 v34, v34\n\t"
    "s_add_u32 s1, s1, 0x4000\n\t"
    "v_exp_f32 v35, v35\n\t"
    "s_and_b32 s1, s1, 0x1ffff\n\t"
    "v_exp_f32 v36, v36\n\t"
    "s_add_u32 m0, s46, 16384\n\t"
    "v_exp_f32 v37, v37\n\t"
    "v_mfma_f32_32x32x16_bf16 v[48:63], v[100:103], v[96:99], -4.0\n\t"
    "ds_read_b128 v[100:103], v14 offset:12288\n\t"
    "buffer_load_dwordx4 v4, s[20:23], s1 offen sc1 lds\n\t"
    "v_exp_f32 v38, v38\n\t"
    "v_exp_f32 v39, v39\n\t"
    "v_exp_f32 v40, v40\n\t"
    "s_waitcnt lgkmcnt(2)\n\t"
    "v_exp_f32 v41, v41\n\t"
    "v_smfmac_f32_32x32x32_bf16 v[16:31], v[108:111], v[32:39], v15\n\t"
    "buffer_load_dwordx4 v4, s[20:23], s1 offen offset:1024 sc1 lds\n\t"
    "v_exp_f32 v42, v42\n\t"
    "v_exp_f32 v43, v43\n\t"
    "v_exp_f32 v44, v44\n\t"
    "v_exp_f32 v45, v45\n\t"
    "v_exp_f32 v46, v46\n\t"
    "v_exp_f32 v47, v47\n\t"
    "v_exp_f32 v48, v48\n\t"
    "s_waitcnt lgkmcnt(0)\n\t"
    "v_exp_f32 v49, v49\n\t"
    "v_smfmac_f32_32x32x32_bf16 v[16:31], v[112:115], v[40:47], v15\n\t"
    "buffer_store_dwordx4 v[132:135], v4, s[20:23], s99 offen sc1\n\t"
    "v_exp_f32 v50, v50\n\t"
    "v_exp_f32 v51, v51\n\t"
    "v_exp_f32 v52, v52\n\t"
    "v_exp_f32 v53, v53\n\t"
    "v_mfma_f32_32x32x16_bf16 v[32:47], v[100:103], v[104:107], -4.0\n\t"
    "v_exp_f32 v54, v54\n\t"
    "v_exp_f32 v55, v55\n\t"
    "v_exp_f32 v56, v56\n\t"
    "v_exp_f32 v57, v57\n\t"
    "v_smfmac_f32_32x32x32_bf16 v[116:131], v[108:111], v[48:55], v15\n\t"
    "v_exp_f32 v58, v58\n\t"
    "v_exp_f32 v59, v59\n\t"
    "v_exp_f32 v60, v60\n\t"
    "v_exp_f32 v61, v61\n\t"
    "v_exp_f32 v62, v62\n\t"
    "v_exp_f32 v63, v63\n\t"
    "s_nop 0\n\t"
    "v_exp_f32 v32, v32\n\t"
    "v_exp_f32 v33, v33\n\t"
    "v_smfmac_f32_32x32x32_bf16 v[116:131], v[112:115], v[56:63], v15\n\t"
    "s_mov_b64 exec, s[66:67]\n\t"
    "ds_read_b128 v[108:111], v5 offset:12288\n\t"
    "ds_read_b128 v[112:115], v5 offset:12800\n\t"
    "s_mov_b64 exec, -1\n\t"
    "v_exp_f32 v34, v34\n\t"
    "v_exp_f32 v35, v35\n\t"
    "v_exp_f32 v36, v36\n\t"
    "v_exp_f32 v37, v37\n\t"
    "v_mfma_f32_32x32x16_bf16 v[48:63], v[100:103], v[96:99], -4.0\n\t"
    "ds_read_b128 v[100:103], v14 offset:16384\n\t"
    "v_exp_f32 v38, v38\n\t"
    "v_exp_f32 v39, v39\n\t"
    "v_exp_f32 v40, v40\n\t"
    "s_waitcnt lgkmcnt(2)\n\t"
    "v_exp_f32 v41, v41\n\t"
    "v_smfmac_f32_32x32x32_bf16 v[16:31], v[108:111], v[32:39], v15\n\t"
    "v_exp_f32 v42, v42\n\t"
    "v_exp_f32 v43, v43\n\t"
    "v_exp_f32 v44, v44\n\t"
    "v_exp_f32 v45, v45\n\t"
    "v_exp_f32 v46, v46\n\t"
    "v_exp_f32 v47, v47\n\t"
    "v_exp_f32 v48, v48\n\t"
    "s_waitcnt lgkmcnt(0)\n\t"
    "v_exp_f32 v49, v49\n\t"
    "v_smfmac_f32_32x32x32_bf16 v[16:31], v[112:115], v[40:47], v15\n\t"
    "v_exp_f32 v50, v50\n\t"
    "v_exp_f32 v51, v51\n\t"
    "v_exp_f32 v52, v52\n\t"
    "v_exp_f32 v53, v53\n\t"
    "v_mfma_f32_32x32x16_bf16 v[32:47], v[100:103], v[104:107], -4.0\n\t"
    "v_exp_f32 v54, v54\n\t"
    "v_exp_f32 v55, v55\n\t"
    "v_exp_f32 v56, v56\n\t"
    "v_exp_f32 v57, v57\n\t"
    "v_smfmac_f32_32x32x32_bf16 v[116:131], v[108:111], v[48:55], v15\n\t"
    "v_exp_f32 v58, v58\n\t"
    "v_exp_f32 v59, v59\n\t"
    "v_exp_f32 v60, v60\n\t"
    "v_exp_f32 v61, v61\n\t"
    "v_exp_f32 v62, v62\n\t"
    "v_exp_f32 v63, v63\n\t"
    "s_nop 0\n\t"
    "v_exp_f32 v32, v32\n\t"
    "v_exp_f32 v33, v33\n\t"
    "v_smfmac_f32_32x32x32_bf16 v[116:131], v[112:115], v[56:63], v15\n\t"
    "s_mov_b64 exec, s[66:67]\n\t"
    "ds_read_b128 v[108:111], v5 offset:16384\n\t"
    "ds_read_b128 v[112:115], v5 offset:16896\n\t"
    "s_mov_b64 exec, -1\n\t"
    "v_exp_f32 v34, v34\n\t"
    "v_exp_f32 v35, v35\n\t"
    "v_exp_f32 v36, v36\n\t"
    "v_exp_f32 v37, v37\n\t"
    "v_mfma_f32_32x32x16_bf16 v[48:63], v[100:103], v[96:99], -4.0\n\t"
    "ds_read_b128 v[100:103], v14 offset:20480\n\t"
    "v_exp_f32 v38, v38\n\t"
    "v_exp_f32 v39, v39\n\t"
    "v_exp_f32 v40, v40\n\t"
    "s_waitcnt lgkmcnt(2)\n\t"
    "v_exp_f32 v41, v41\n\t"
    "v_smfmac_f32_32x32x32_bf16 v[16:31], v[108:111], v[32:39], v15\n\t"
    "v_exp_f32 v42, v42\n\t"
    "v_exp_f32 v43, v43\n\t"
    "v_exp_f32 v44, v44\n\t"
    "v_exp_f32 v45, v45\n\t"
    "v_exp_f32 v46, v46\n\t"
    "s_setprio 2\n\t"
    "v_exp_f32 v47, v47\n\t"
    "v_exp_f32 v48, v48\n\t"
    "s_waitcnt lgkmcnt(0)\n\t"
    "v_exp_f32 v49, v49\n\t"
    "v_smfmac_f32_32x32x32_bf16 v[16:31], v[112:115], v[40:47], v15\n\t"
    "v_exp_f32 v50, v50\n\t"
    "v_exp_f32 v51, v51\n\t"
    "v_exp_f32 v52, v52\n\t"
    "v_exp_f32 v53, v53\n\t"
    "v_mfma_f32_32x32x16_bf16 v[32:47], v[100:103], v[104:107], -4.0\n\t"
    "v_exp_f32 v54, v54\n\t"
    "v_exp_f32 v55, v55\n\t"
    "v_exp_f32 v56, v56\n\t"
    "v_exp_f32 v57, v57\n\t"
    "v_smfmac_f32_32x32x32_bf16 v[116:131], v[108:111], v[48:55], v15\n\t"
    "v_exp_f32 v58, v58\n\t"
    "v_exp_f32 v59, v59\n\t"
    "v_exp_f32 v60, v60\n\t"
    "v_exp_f32 v61, v61\n\t"
    "v_exp_f32 v62, v62\n\t"
    "v_exp_f32 v63, v63\n\t"
    "s_nop 0\n\t"
    "v_exp_f32 v32, v32\n\t"
    "v_exp_f32 v33, v33\n\t"
    "v_smfmac_f32_32x32x32_bf16 v[116:131], v[112:115], v[56:63], v15\n\t"
    "s_mov_b64 exec, s[66:67]\n\t"
    "ds_read_b128 v[108:111], v5 offset:20480\n\t"
    "ds_read_b128 v[112:115], v5 offset:20992\n\t"
    "s_mov_b64 exec, -1\n\t"
    "v_exp_f32 v34, v34\n\t"
    "v_exp_f32 v35, v35\n\t"
    "v_exp_f32 v36, v36\n\t"
    "v_exp_f32 v37, v37\n\t"
    "v_mfma_f32_32x32x16_bf16 v[48:63], v[100:103], v[96:99], -4.0\n\t"
    "ds_read_b128 v[100:103], v14 offset:24576\n\t"
    "v_exp_f32 v38, v38\n\t"
    "v_exp_f32 v39, v39\n\t"
    "v_exp_f32 v40, v40\n\t"
    "s_waitcnt lgkmcnt(2)\n\t"
    "v_exp_f32 v41, v41\n\t"
    "v_smfmac_f32_32x32x32_bf16 v[16:31], v[108:111], v[32:39], v15\n\t"
    "v_exp_f32 v42, v42\n\t"
    "v_exp_f32 v43, v43\n\t"
    "v_exp_f32 v44, v44\n\t"
    "v_exp_f32 v45, v45\n\t"
    "v_exp_f32 v46, v46\n\t"
    "v_exp_f32 v47, v47\n\t"
    "v_exp_f32 v48, v48\n\t"
    "s_waitcnt vmcnt(0) lgkmcnt(0)\n\t"
    "v_exp_f32 v49, v49\n\t"
    "v_smfmac_f32_32x32x32_bf16 v[16:31], v[112:115], v[40:47], v15\n\t"
    "s_mov_b64 exec, 1\n\t"
    "buffer_store_dwordx2 v[12:13], off, s[20:23], s3 sc1\n\t"
    "s_mov_b64 exec, -1\n\t"
    "v_exp_f32 v50, v50\n\t"
    "v_exp_f32 v51, v51\n\t"
    "v_exp_f32 v52, v52\n\t"
    "v_exp_f32 v53, v53\n\t"
    "v_mfma_f32_32x32x16_bf16 v[32:47], v[100:103], v[104:107], -4.0\n\t"
    "global_load_dword v142, v140, s[36:37]\n\t"
    "global_load_dword v143, v141, s[32:33]\n\t"
    "v_exp_f32 v54, v54\n\t"
    "v_exp_f32 v55, v55\n\t"
    "v_exp_f32 v56, v56\n\t"
    "v_exp_f32 v57, v57\n\t"
    "v_smfmac_f32_32x32x32_bf16 v[116:131], v[108:111], v[48:55], v15\n\t"
    "v_exp_f32 v58, v58\n\t"
    "v_exp_f32 v59, v59\n\t"
    "v_exp_f32 v60, v60\n\t"
    "v_exp_f32 v61, v61\n\t"
    "v_exp_f32 v62, v62\n\t"
    "s_setprio 1\n\t"
    "v_exp_f32 v63, v63\n\t"
    "v_exp_f32 v32, v32\n\t"
    "v_exp_f32 v33, v33\n\t"
    "v_smfmac_f32_32x32x32_bf16 v[116:131], v[112:115], v[56:63], v15\n\t"
    "s_mov_b64 exec, s[66:67]\n\t"
    "ds_read_b128 v[108:111], v5 offset:24576\n\t"
    "ds_read_b128 v[112:115], v5 offset:25088\n\t"
    "s_mov_b64 exec, -1\n\t"
    "v_exp_f32 v34, v34\n\t"
    "v_exp_f32 v35, v35\n\t"
    "v_exp_f32 v36, v36\n\t"
    "v_exp_f32 v37, v37\n\t"
    "v_mfma_f32_32x32x16_bf16 v[48:63], v[100:103], v[96:99], -4.0\n\t"
    "ds_read_b128 v[100:103], v14 offset:28672\n\t"
    "v_exp_f32 v38, v38\n\t"
    "s_add_u32 m0, s46, 131072\n\t"
    "v_exp_f32 v39, v39\n\t"
    "v_exp_f32 v40, v40\n\t"
    "s_waitcnt lgkmcnt(2)\n\t"
    "v_exp_f32 v41, v41\n\t"
    "v_smfmac_f32_32x32x32_bf16 v[16:31], v[108:111], v[32:39], v15\n\t"
    "v_exp_f32 v42, v42\n\t"
    "v_exp_f32 v43, v43\n\t"
    "v_exp_f32 v44, v44\n\t"
    "v_exp_f32 v45, v45\n\t"
    "v_exp_f32 v46, v46\n\t"
    "v_exp_f32 v47, v47\n\t"
    "v_exp_f32 v48, v48\n\t"
    "s_waitcnt lgkmcnt(0)\n\t"
    "v_exp_f32 v49, v49\n\t"
    "v_smfmac_f32_32x32x32_bf16 v[16:31], v[112:115], v[40:47], v15\n\t"
    "global_load_lds_dwordx4 v8, s[36:37]\n\t"
    "v_exp_f32 v50, v50\n\t"
    "v_exp_f32 v51, v51\n\t"
    "v_exp_f32 v52, v52\n\t"
    "v_exp_f32 v53, v53\n\t"
    "v_mfma_f32_32x32x16_bf16 v[32:47], v[100:103], v[104:107], -4.0\n\t"
    "global_load_lds_dwordx4 v9, s[36:37] offset:1024\n\t"
    "v_exp_f32 v54, v54\n\t"
    "v_exp_f32 v55, v55\n\t"
    "v_exp_f32 v56, v56\n\t"
    "v_exp_f32 v57, v57\n\t"
    "v_smfmac_f32_32x32x32_bf16 v[116:131], v[108:111], v[48:55], v15\n\t"
    "v_exp_f32 v58, v58\n\t"
    "v_exp_f32 v59, v59\n\t"
    "v_exp_f32 v60, v60\n\t"
    "v_exp_f32 v61, v61\n\t"
    "v_exp_f32 v62, v62\n\t"
    "v_exp_f32 v63, v63\n\t"
    "s_barrier\n\t"
    "s_setprio 3\n\t"
    "v_exp_f32 v32, v32\n\t"
    "v_exp_f32 v33, v33\n\t"
    "v_smfmac_f32_32x32x32_bf16 v[116:131], v[112:115], v[56:63], v15\n\t"
    "s_mov_b64 exec, s[66:67]\n\t"
    "ds_read_b128 v[108:111], v5 offset:28672\n\t"
    "ds_read_b128 v[112:115], v5 offset:29184\n\t"
    "s_mov_b64 exec, -1\n\t"
    "v_exp_f32 v34, v34\n\t"
    "v_exp_f32 v35, v35\n\t"
    "v_exp_f32 v36, v36\n\t"
    "v_exp_f32 v37, v37\n\t"
    "v_mfma_f32_32x32x16_bf16 v[48:63], v[100:103], v[96:99], -4.0\n\t"
    "ds_read_b128 v[100:103], v14 offset:32768\n\t"
    "buffer_load_dwordx2 v[132:133], v7, s[20:23], 0 offen sc1\n\t"
    "v_exp_f32 v38, v38\n\t"
    "s_add_u32 s36, s36, 0x4000\n\t"
    "v_exp_f32 v39, v39\n\t"
    "s_addc_u32 s37, s37, 0\n\t"
    "v_exp_f32 v40, v40\n\t"
    "s_waitcnt lgkmcnt(2)\n\t"
    "v_exp_f32 v41, v41\n\t"
    "v_smfmac_f32_32x32x32_bf16 v[16:31], v[108:111], v[32:39], v15\n\t"
    "v_exp_f32 v42, v42\n\t"
    "s_add_u32 m0, s46, 147456\n\t"
    "v_exp_f32 v43, v43\n\t"
    "v_exp_f32 v44, v44\n\t"
    "v_exp_f32 v45, v45\n\t"
    "v_exp_f32 v46, v46\n\t"
    "v_exp_f32 v47, v47\n\t"
    "v_exp_f32 v48, v48\n\t"
    "s_waitcnt lgkmcnt(0)\n\t"
    "v_exp_f32 v49, v49\n\t"
    "v_smfmac_f32_32x32x32_bf16 v[16:31], v[112:115], v[40:47], v15\n\t"
    "global_load_lds_dwordx4 v8, s[36:37]\n\t"
    "v_exp_f32 v50, v50\n\t"
    "v_exp_f32 v51, v51\n\t"
    "v_exp_f32 v52, v52\n\t"
    "v_exp_f32 v53, v53\n\t"
    "v_mfma_f32_32x32x16_bf16 v[32:47], v[100:103], v[104:107], -4.0\n\t"
    "global_load_lds_dwordx4 v9, s[36:37] offset:1024\n\t"
    "v_exp_f32 v54, v54\n\t"
    "v_exp_f32 v55, v55\n\t"
    "v_exp_f32 v56, v56\n\t"
    "v_exp_f32 v57, v57\n\t"
    "v_smfmac_f32_32x32x32_bf16 v[116:131], v[108:111], v[48:55], v15\n\t"
    "v_exp_f32 v58, v58\n\t"
    "v_exp_f32 v59, v59\n\t"
    "v_exp_f32 v60, v60\n\t"
    "v_exp_f32 v61, v61\n\t"
    "v_exp_f32 v62, v62\n\t"
    "v_exp_f32 v63, v63\n\t"
    "v_exp_f32 v32, v32\n\t"
    "s_waitcnt vmcnt(2)\n\t"
    "v_cmp_eq_u64 vcc, v[12:13], v[132:133]\n\t"
    "v_exp_f32 v33, v33\n\t"
    "v_smfmac_f32_32x32x32_bf16 v[116:131], v[112:115], v[56:63], v15\n\t"
    "s_mov_b64 exec, s[66:67]\n\t"
    "ds_read_b128 v[108:111], v5 offset:32768\n\t"
    "ds_read_b128 v[112:115], v5 offset:33280\n\t"
    "s_mov_b64 exec, -1\n\t"
    "v_exp_f32 v34, v34\n\t"
    "s_cmp_eq_u64 vcc, -1\n\t"
    "v_exp_f32 v35, v35\n\t"
    "s_cbranch_scc0 .Lfmm_spin\n\t"
    ".Lfmm_spun:\n\t"
    "v_exp_f32 v36, v36\n\t"
    "v_exp_f32 v37, v37\n\t"
    "v_mfma_f32_32x32x16_bf16 v[48:63], v[100:103], v[96:99], -4.0\n\t"
    "ds_read_b128 v[100:103], v14 offset:36864\n\t"
    "global_load_dwordx4 v[88:91], v4, s[52:53] offset:-4096 sc1\n\t"
    "v_exp_f32 v38, v38\n\t"
    "v_exp_f32 v39, v39\n\t"
    "v_exp_f32 v40, v40\n\t"
    "s_waitcnt lgkmcnt(2)\n\t"
    "v_exp_f32 v41, v41\n\t"
    "v_smfmac_f32_32x32x32_bf16 v[16:31], v[108:111], v[32:39], v15\n\t"
    "global_load_dwordx4 v[92:95], v4, s[52:53] offset:-3072 sc1\n\t"
    "v_exp_f32 v42, v42\n\t"
    "v_exp_f32 v43, v43\n\t"
    "v_exp_f32 v44, v44\n\t"
    "v_exp_f32 v45, v45\n\t"
    "v_exp_f32 v46, v46\n\t"
    "v_exp_f32 v47, v47\n\t"
    "v_exp_f32 v48, v48\n\t"
    "s_waitcnt lgkmcnt(0)\n\t"
    "v_exp_f32 v49, v49\n\t"
    "v_smfmac_f32_32x32x32_bf16 v[16:31], v[112:115], v[40:47], v15\n\t"
    "v_exp_f32 v50, v50\n\t"
    "v_exp_f32 v51, v51\n\t"
    "v_exp_f32 v52, v52\n\t"
    "v_exp_f32 v53, v53\n\t"
    "v_mfma_f32_32x32x16_bf16 v[32:47], v[100:103], v[104:107], -4.0\n\t"
    "ds_read_b64_tr_b16 v[64:65], v10 offset:0\n\t"
    "ds_read_b64_tr_b16 v[66:67], v10 offset:256\n\t"
    "v_exp_f32 v54, v54\n\t"
    "v_exp_f32 v55, v55\n\t"
    "v_exp_f32 v56, v56\n\t"
    "v_exp_f32 v57, v57\n\t"
    "v_smfmac_f32_32x32x32_bf16 v[116:131], v[108:111], v[48:55], v15\n\t"
    "ds_read_b64_tr_b16 v[68:69], v10 offset:512\n\t"
    "ds_read_b64_tr_b16 v[70:71], v10 offset:768\n\t"
    "v_exp_f32 v58, v58\n\t"
    "v_exp_f32 v59, v59\n\t"
    "v_exp_f32 v60, v60\n\t"
    "v_exp_f32 v61, v61\n\t"
    "v_exp_f32 v62, v62\n\t"
    "v_exp_f32 v63, v63\n\t"
    "v_exp_f32 v32, v32\n\t"
    "v_exp_f32 v33, v33\n\t"
    "v_smfmac_f32_32x32x32_bf16 v[116:131], v[112:115], v[56:63], v15\n\t"
    "s_mov_b64 exec, s[66:67]\n\t"
    "ds_read_b128 v[108:111], v5 offset:36864\n\t"
    "ds_read_b128 v[112:115], v5 offset:37376\n\t"
    "s_mov_b64 exec, -1\n\t"
    "v_exp_f32 v34, v34\n\t"
    "v_exp_f32 v35, v35\n\t"
    "v_exp_f32 v36, v36\n\t"
    "v_exp_f32 v37, v37\n\t"
    "v_mfma_f32_32x32x16_bf16 v[48:63], v[100:103], v[96:99], -4.0\n\t"
    "ds_read_b128 v[100:103], v14 offset:40960\n\t"
    "global_load_dwordx4 v[232:235], v4, s[52:53] offset:-2048 sc1\n\t"
    "v_exp_f32 v38, v38\n\t"
    "s_add_u32 m0, s46, 131072\n\t"
    "v_exp_f32 v39, v39\n\t"
    "v_exp_f32 v40, v40\n\t"
    "s_waitcnt lgkmcnt(2)\n\t"
    "v_exp_f32 v41, v41\n\t"
    "v_smfmac_f32_32x32x32_bf16 v[16:31], v[108:111], v[32:39], v15\n\t"
    "global_load_dwordx4 v[236:239], v4, s[52:53] offset:-1024 sc1\n\t"
    "v_exp_f32 v42, v42\n\t"
    "v_exp_f32 v43, v43\n\t"
    "v_exp_f32 v44, v44\n\t"
    "v_exp_f32 v45, v45\n\t"
    "v_exp_f32 v46, v46\n\t"
    "v_exp_f32 v47, v47\n\t"
    "v_exp_f32 v48, v48\n\t"
    "s_waitcnt lgkmcnt(0)\n\t"
    "v_exp_f32 v49, v49\n\t"
    "v_smfmac_f32_32x32x32_bf16 v[16:31], v[112:115], v[40:47], v15\n\t"
    "global_load_lds_dwordx4 v8, s[32:33]\n\t"
    "v_exp_f32 v50, v50\n\t"
    "v_exp_f32 v51, v51\n\t"
    "v_exp_f32 v52, v52\n\t"
    "v_exp_f32 v53, v53\n\t"
    "v_mfma_f32_32x32x16_bf16 v[32:47], v[100:103], v[104:107], -4.0\n\t"
    "global_load_lds_dwordx4 v9, s[32:33] offset:1024\n\t"
    "v_exp_f32 v54, v54\n\t"
    "v_exp_f32 v55, v55\n\t"
    "v_exp_f32 v56, v56\n\t"
    "v_exp_f32 v57, v57\n\t"
    "v_smfmac_f32_32x32x32_bf16 v[116:131], v[108:111], v[48:55], v15\n\t"
    "v_exp_f32 v58, v58\n\t"
    "v_exp_f32 v59, v59\n\t"
    "v_exp_f32 v60, v60\n\t"
    "v_exp_f32 v61, v61\n\t"
    "v_exp_f32 v62, v62\n\t"
    "v_exp_f32 v63, v63\n\t"
    "v_exp_f32 v32, v32\n\t"
    "v_exp_f32 v33, v33\n\t"
    "v_smfmac_f32_32x32x32_bf16 v[116:131], v[112:115], v[56:63], v15\n\t"
    "s_mov_b64 exec, s[66:67]\n\t"
    "ds_read_b128 v[108:111], v5 offset:40960\n\t"
    "ds_read_b128 v[112:115], v5 offset:41472\n\t"
    "s_mov_b64 exec, -1\n\t"
    "v_exp_f32 v34, v34\n\t"
    "v_exp_f32 v35, v35\n\t"
    "v_exp_f32 v36, v36\n\t"
    "v_exp_f32 v37, v37\n\t"
    "v_mfma_f32_32x32x16_bf16 v[48:63], v[100:103], v[96:99], -4.0\n\t"
    "ds_read_b128 v[100:103], v14 offset:45056\n\t"
    "global_load_dwordx4 v[240:243], v4, s[52:53] offset:0 sc1\n\t"
    "v_exp_f32 v38, v38\n\t"
    "v_exp_f32 v39, v39\n\t"
    "v_exp_f32 v40, v40\n\t"
    "s_waitcnt vmcnt(5) lgkmcnt(2)\n\t"
    "v_exp_f32 v41, v41\n\t"
    "v_smfmac_f32_32x32x32_bf16 v[16:31], v[108:111], v[32:39], v15\n\t"
    "global_load_dwordx4 v[244:247], v4, s[52:53] offset:1024 sc1\n\t"
    "v_exp_f32 v42, v42\n\t"
    "v_exp_f32 v43, v43\n\t"
    "v_exp_f32 v44, v44\n\t"
    "v_exp_f32 v45, v45\n\t"
    "v_mfma_f32_32x32x16_bf16 v[136:151], v[64:67], v[88:91], 0\n\t"
    "ds_read_b64_tr_b16 v[72:73], v10 offset:16384\n\t"
    "v_exp_f32 v46, v46\n\t"
    "v_exp_f32 v47, v47\n\t"
    "v_exp_f32 v48, v48\n\t"
    "s_waitcnt lgkmcnt(1)\n\t"
    "v_exp_f32 v49, v49\n\t"
    "v_smfmac_f32_32x32x32_bf16 v[16:31], v[112:115], v[40:47], v15\n\t"
    "ds_read_b64_tr_b16 v[74:75], v10 offset:16640\n\t"
    "ds_read_b64_tr_b16 v[76:77], v10 offset:16896\n\t"
    "v_exp_f32 v50, v50\n\t"
    "v_exp_f32 v51, v51\n\t"
    "v_exp_f32 v52, v52\n\t"
    "v_exp_f32 v53, v53\n\t"
    "v_mfma_f32_32x32x16_bf16 v[32:47], v[100:103], v[104:107], -4.0\n\t"
    "ds_read_b64_tr_b16 v[78:79], v10 offset:17152\n\t"
    "v_exp_f32 v54, v54\n\t"
    "s_add_u32 s36, s36, 0x4000\n\t"
    "v_exp_f32 v55, v55\n\t"
    "s_addc_u32 s37, s37, 0\n\t"
    "v_exp_f32 v56, v56\n\t"
    "v_exp_f32 v57, v57\n\t"
    "v_smfmac_f32_32x32x32_bf16 v[116:131], v[108:111], v[48:55], v15\n\t"
    "v_exp_f32 v58, v58\n\t"
    "s_add_u32 m0, s46, 147456\n\t"
    "v_exp_f32 v59, v59\n\t"
    "v_exp_f32 v60, v60\n\t"
    "v_exp_f32 v61, v61\n\t"
    "v_mfma_f32_32x32x16_bf16 v[152:167], v[64:67], v[92:95], 0\n\t"
    "v_exp_f32 v62, v62\n\t"
    "v_exp_f32 v63, v63\n\t"
    "v_exp_f32 v32, v32\n\t"
    "s_waitcnt lgkmcnt(0)\n\t"
    "v_exp_f32 v33, v33\n\t"
    "v_smfmac_f32_32x32x32_bf16 v[116:131], v[112:115], v[56:63], v15\n\t"
    "s_mov_b64 exec, s[66:67]\n\t"
    "ds_read_b128 v[108:111], v5 offset:45056\n\t"
    "ds_read_b128 v[112:115], v5 offset:45568\n\t"
    "s_mov_b64 exec, -1\n\t"
    "v_exp_f32 v34, v34\n\t"
    "v_exp_f32 v35, v35\n\t"
    "v_exp_f32 v36, v36\n\t"
    "v_exp_f32 v37, v37\n\t"
    "v_mfma_f32_32x32x16_bf16 v[48:63], v[100:103], v[96:99], -4.0\n\t"
    "ds_read_b128 v[100:103], v14 offset:49152\n\t"
    "global_load_lds_dwordx4 v8, s[36:37]\n\t"
    "v_exp_f32 v38, v38\n\t"
    "v_exp_f32 v39, v39\n\t"
    "v_exp_f32 v40, v40\n\t"
    "s_waitcnt vmcnt(3) lgkmcnt(2)\n\t"
    "v_exp_f32 v41, v41\n\t"
    "v_smfmac_f32_32x32x32_bf16 v[16:31], v[108:111], v[32:39], v15\n\t"
    "global_load_lds_dwordx4 v9, s[36:37] offset:1024\n\t"
    "global_load_dwordx4 v[248:251], v4, s[52:53] offset:2048 sc1\n\t"
    "v_exp_f32 v42, v42\n\t"
    "v_exp_f32 v43, v43\n\t"
    "v_exp_f32 v44, v44\n\t"
    "v_exp_f32 v45, v45\n\t"
    "v_mfma_f32_32x32x16_bf16 v[168:183], v[68:71], v[88:91], 0\n\t"
    "ds_read_b64_tr_b16 v[200:201], v10 offset:0\n\t"
    "v_exp_f32 v46, v46\n\t"
    "v_exp_f32 v47, v47\n\t"
    "v_exp_f32 v48, v48\n\t"
    "s_waitcnt lgkmcnt(1)\n\t"
    "v_exp_f32 v49, v49\n\t"
    "v_smfmac_f32_32x32x32_bf16 v[16:31], v[112:115], v[40:47], v15\n\t"
    "ds_read_b64_tr_b16 v[202:203], v10 offset:256\n\t"
    "ds_read_b64_tr_b16 v[204:205], v10 offset:512\n\t"
    "v_exp_f32 v50, v50\n\t"
    "v_exp_f32 v51, v51\n\t"
    "v_exp_f32 v52, v52\n\t"
    "v_exp_f32 v53, v53\n\t"
    "v_mfma_f32_32x32x16_bf16 v[32:47], v[100:103], v[104:107], -4.0\n\t"
    "ds_read_b64_tr_b16 v[206:207], v10 offset:768\n\t"
    "global_load_dwordx4 v[252:255], v4, s[52:53] offset:3072 sc1\n\t"
    "v_exp_f32 v54, v54\n\t"
    "s_add_u32 s36, s36, 0x4000\n\t"
    "v_exp_f32 v55, v55\n\t"
    "s_addc_u32 s37, s37, 0\n\t"
    "v_exp_f32 v56, v56\n\t"
    "v_exp_f32 v57, v57\n\t"
    "v_smfmac_f32_32x32x32_bf16 v[116:131], v[108:111], v[48:55], v15\n\t"
    "v_exp_f32 v58, v58\n\t"
    "s_add_u32 m0, s46, 131072\n\t"
    "v_exp_f32 v59, v59\n\t"
    "v_exp_f32 v60, v60\n\t"
    "v_exp_f32 v61, v61\n\t"
    "v_mfma_f32_32x32x16_bf16 v[184:199], v[68:71], v[92:95], 0\n\t"
    "v_exp_f32 v62, v62\n\t"
    "v_exp_f32 v63, v63\n\t"
    "v_exp_f32 v32, v32\n\t"
    "s_waitcnt lgkmcnt(0)\n\t"
    "v_exp_f32 v33, v33\n\t"
    "v_smfmac_f32_32x32x32_bf16 v[116:131], v[112:115], v[56:63], v15\n\t"
    "s_mov_b64 exec, s[66:67]\n\t"
    "ds_read_b128 v[108:111], v5 offset:49152\n\t"
    "ds_read_b128 v[112:115], v5 offset:49664\n\t"
    "s_mov_b64 exec, -1\n\t"
    "v_exp_f32 v34, v34\n\t"
    "s_add_u32 s52, s52, 0x2000\n\t"
    "v_exp_f32 v35, v35\n\t"
    "s_addc_u32 s53, s53, 0\n\t"
    "v_exp_f32 v36, v36\n\t"
    "v_exp_f32 v37, v37\n\t"
    "v_mfma_f32_32x32x16_bf16 v[48:63], v[100:103], v[96:99], -4.0\n\t"
    "ds_read_b128 v[100:103], v14 offset:53248\n\t"
    "global_load_lds_dwordx4 v8, s[36:37]\n\t"
    "v_exp_f32 v38, v38\n\t"
    "v_exp_f32 v39, v39\n\t"
    "v_exp_f32 v40, v40\n\t"
    "s_waitcnt lgkmcnt(2)\n\t"
    "v_exp_f32 v41, v41\n\t"
    "v_smfmac_f32_32x32x32_bf16 v[16:31], v[108:111], v[32:39], v15\n\t"
    "global_load_lds_dwordx4 v9, s[36:37] offset:1024\n\t"
    "global_load_dwordx4 v[80:83], v4, s[52:53] offset:-4096 sc1\n\t"
    "v_exp_f32 v42, v42\n\t"
    "v_exp_f32 v43, v43\n\t"
    "v_exp_f32 v44, v44\n\t"
    "v_exp_f32 v45, v45\n\t"
    "v_mfma_f32_32x32x16_bf16 v[136:151], v[72:75], v[232:235], v[136:151]\n\t"
    "global_load_dwordx4 v[84:87], v4, s[52:53] offset:-3072 sc1\n\t"
    "v_exp_f32 v46, v46\n\t"
    "v_exp_f32 v47, v47\n\t"
    "v_exp_f32 v48, v48\n\t"
    "s_waitcnt lgkmcnt(0)\n\t"
    "v_exp_f32 v49, v49\n\t"
    "v_smfmac_f32_32x32x32_bf16 v[16:31], v[112:115], v[40:47], v15\n\t"
    "v_exp_f32 v50, v50\n\t"
    "v_exp_f32 v51, v51\n\t"
    "v_exp_f32 v52, v52\n\t"
    "v_exp_f32 v53, v53\n\t"
    "v_mfma_f32_32x32x16_bf16 v[32:47], v[100:103], v[104:107], -4.0\n\t"
    "v_exp_f32 v54, v54\n\t"
    "v_exp_f32 v55, v55\n\t"
    "v_exp_f32 v56, v56\n\t"
    "s_waitcnt vmcnt(6)\n\t"
    "v_exp_f32 v57, v57\n\t"
    "v_smfmac_f32_32x32x32_bf16 v[116:131], v[108:111], v[48:55], v15\n\t"
    "v_exp_f32 v58, v58\n\t"
    "v_exp_f32 v59, v59\n\t"
    "v_exp_f32 v60, v60\n\t"
    "v_exp_f32 v61, v61\n\t"
    "v_mfma_f32_32x32x16_bf16 v[152:167], v[72:75], v[236:239], v[152:167]\n\t"
    "ds_read_b64_tr_b16 v[64:65], v10 offset:16384\n\t"
    "v_exp_f32 v62, v62\n\t"
    "v_exp_f32 v63, v63\n\t"
    "v_exp_f32 v32, v32\n\t"
    "v_exp_f32 v33, v33\n\t"
    "v_smfmac_f32_32x32x32_bf16 v[116:131], v[112:115], v[56:63], v15\n\t"
    "s_mov_b64 exec, s[66:67]\n\t"
    "ds_read_b128 v[108:111], v5 offset:53248\n\t"
    "ds_read_b128 v[112:115], v5 offset:53760\n\t"
    "s_mov_b64 exec, -1\n\t"
    "v_exp_f32 v34, v34\n\t"
    "v_exp_f32 v35, v35\n\t"
    "v_exp_f32 v36, v36\n\t"
    "v_exp_f32 v37, v37\n\t"
    "v_mfma_f32_32x32x16_bf16 v[48:63], v[100:103], v[96:99], -4.0\n\t"
    "ds_read_b128 v[100:103], v14 offset:57344\n\t"
    "ds_read_b64_tr_b16 v[66:67], v10 offset:16640\n\t"
    "v_exp_f32 v38, v38\n\t"
    "s_add_u32 s32, s32, 0x4000\n\t"
    "v_exp_f32 v39, v39\n\t"
    "s_addc_u32 s33, s33, 0\n\t"
    "v_exp_f32 v40, v40\n\t"
    "s_waitcnt lgkmcnt(3)\n\t"
    "v_exp_f32 v41, v41\n\t"
    "v_smfmac_f32_32x32x32_bf16 v[16:31], v[108:111], v[32:39], v15\n\t"
    "ds_read_b64_tr_b16 v[68:69], v10 offset:16896\n\t"
    "ds_read_b64_tr_b16 v[70:71], v10 offset:17152\n\t"
    "v_exp_f32 v42, v42\n\t"
    "s_add_u32 m0, s46, 147456\n\t"
    "v_exp_f32 v43, v43\n\t"
    "v_exp_f32 v44, v44\n\t"
    "v_exp_f32 v45, v45\n\t"
    "v_mfma_f32_32x32x16_bf16 v[168:183], v[76:79], v[232:235], v[168:183]\n\t"
    "global_load_dwordx4 v[88:91], v4, s[52:53] offset:-2048 sc1\n\t"
    "v_exp_f32 v46, v46\n\t"
    "v_exp_f32 v47, v47\n\t"
    "v_exp_f32 v48, v48\n\t"
    "s_waitcnt lgkmcnt(0)\n\t"
    "v_exp_f32 v49, v49\n\t"
    "v_smfmac_f32_32x32x32_bf16 v[16:31], v[112:115], v[40:47], v15\n\t"
    "global_load_lds_dwordx4 v8, s[32:33]\n\t"
    "global_load_dwordx4 v[92:95], v4, s[52:53] offset:-1024 sc1\n\t"
    "v_exp_f32 v50, v50\n\t"
    "v_exp_f32 v51, v51\n\t"
    "v_exp_f32 v52, v52\n\t"
    "v_exp_f32 v53, v53\n\t"
    "v_mfma_f32_32x32x16_bf16 v[32:47], v[100:103], v[104:107], -4.0\n\t"
    "global_load_lds_dwordx4 v9, s[32:33] offset:1024\n\t"
    "v_exp_f32 v54, v54\n\t"
    "v_exp_f32 v55, v55\n\t"
    "v_exp_f32 v56, v56\n\t"
    "v_exp_f32 v57, v57\n\t"
    "v_smfmac_f32_32x32x32_bf16 v[116:131], v[108:111], v[48:55], v15\n\t"
    "v_exp_f32 v58, v58\n\t"
    "v_exp_f32 v59, v59\n\t"
    "v_exp_f32 v60, v60\n\t"
    "v_exp_f32 v61, v61\n\t"
    "v_mfma_f32_32x32x16_bf16 v[184:199], v[76:79], v[236:239], v[184:199]\n\t"
    "v_exp_f32 v62, v62\n\t"
    "v_exp_f32 v63, v63\n\t"
    "v_exp_f32 v32, v32\n\t"
    "v_exp_f32 v33, v33\n\t"
    "v_smfmac_f32_32x32x32_bf16 v[116:131], v[112:115], v[56:63], v15\n\t"
    "s_mov_b64 exec, s[66:67]\n\t"
    "ds_read_b128 v[108:111], v5 offset:57344\n\t"
    "ds_read_b128 v[112:115], v5 offset:57856\n\t"
    "s_mov_b64 exec, -1\n\t"
    "v_exp_f32 v34, v34\n\t"
    "v_exp_f32 v35, v35\n\t"
    "v_exp_f32 v36, v36\n\t"
    "v_exp_f32 v37, v37\n\t"
    "v_mfma_f32_32x32x16_bf16 v[48:63], v[100:103], v[96:99], -4.0\n\t"
    "ds_read_b128 v[100:103], v14 offset:61440\n\t"
    "global_load_dwordx4 v[232:235], v4, s[52:53] offset:0 sc1\n\t"
    "v_exp_f32 v38, v38\n\t"
    "v_exp_f32 v39, v39\n\t"
    "v_exp_f32 v40, v40\n\t"
    "s_waitcnt vmcnt(7) lgkmcnt(2)\n\t"
    "v_exp_f32 v41, v41\n\t"
    "v_smfmac_f32_32x32x32_bf16 v[16:31], v[108:111], v[32:39], v15\n\t"
    "global_load_dwordx4 v[236:239], v4, s[52:53] offset:1024 sc1\n\t"
    "v_exp_f32 v42, v42\n\t"
    "v_exp_f32 v43, v43\n\t"
    "v_exp_f32 v44, v44\n\t"
    "v_exp_f32 v45, v45\n\t"
    "v_mfma_f32_32x32x16_bf16 v[136:151], v[64:67], v[240:243], v[136:151]\n\t"
    "ds_read_b64_tr_b16 v[72:73], v10 offset:0\n\t"
    "v_exp_f32 v46, v46\n\t"
    "v_exp_f32 v47, v47\n\t"
    "v_exp_f32 v48, v48\n\t"
    "s_waitcnt lgkmcnt(1)\n\t"
    "v_exp_f32 v49, v49\n\t"
    "v_smfmac_f32_32x32x32_bf16 v[16:31], v[112:115], v[40:47], v15\n\t"
    "ds_read_b64_tr_b16 v[74:75], v10 offset:256\n\t"
    "ds_read_b64_tr_b16 v[76:77], v10 offset:512\n\t"
    "v_exp_f32 v50, v50\n\t"
    "v_exp_f32 v51, v51\n\t"
    "v_exp_f32 v52, v52\n\t"
    "v_exp_f32 v53, v53\n\t"
    "v_mfma_f32_32x32x16_bf16 v[32:47], v[100:103], v[104:107], -4.0\n\t"
    "ds_read_b64_tr_b16 v[78:79], v10 offset:768\n\t"
    "v_exp_f32 v54, v54\n\t"
    "s_add_u32 s36, s36, 0x4000\n\t"
    "v_exp_f32 v55, v55\n\t"
    "s_addc_u32 s37, s37, 0\n\t"
    "v_exp_f32 v56, v56\n\t"
    "v_exp_f32 v57, v57\n\t"
    "v_smfmac_f32_32x32x32_bf16 v[116:131], v[108:111], v[48:55], v15\n\t"
    "v_exp_f32 v58, v58\n\t"
    "s_add_u32 m0, s46, 131072\n\t"
    "v_exp_f32 v59, v59\n\t"
    "v_exp_f32 v60, v60\n\t"
    "v_exp_f32 v61, v61\n\t"
    "v_mfma_f32_32x32x16_bf16 v[152:167], v[64:67], v[244:247], v[152:167]\n\t"
    "v_exp_f32 v62, v62\n\t"
    "v_exp_f32 v63, v63\n\t"
    "v_exp_f32 v32, v32\n\t"
    "s_waitcnt lgkmcnt(0)\n\t"
    "v_exp_f32 v33, v33\n\t"
    "v_smfmac_f32_32x32x32_bf16 v[116:131], v[112:115], v[56:63], v15\n\t"
    "s_mov_b64 exec, s[66:67]\n\t"
    "ds_read_b128 v[108:111], v5 offset:61440\n\t"
    "ds_read_b128 v[112:115], v5 offset:61952\n\t"
    "s_mov_b64 exec, -1\n\t"
    "v_exp_f32 v34, v34\n\t"
    "v_exp_f32 v35, v35\n\t"
    "v_exp_f32 v36, v36\n\t"
    "v_exp_f32 v37, v37\n\t"
    "v_mfma_f32_32x32x16_bf16 v[48:63], v[100:103], v[96:99], -4.0\n\t"
    "v_add_u32 v14, 65536, v14\n\t"
    "ds_read_b128 v[100:103], v14 offset:0\n\t"
    "global_load_lds_dwordx4 v8, s[36:37]\n\t"
    "v_exp_f32 v38, v38\n\t"
    "v_exp_f32 v39, v39\n\t"
    "v_exp_f32 v40, v40\n\t"
    "s_waitcnt vmcnt(3) lgkmcnt(2)\n\t"
    "v_exp_f32 v41, v41\n\t"
    "v_smfmac_f32_32x32x32_bf16 v[16:31], v[108:111], v[32:39], v15\n\t"
    "global_load_lds_dwordx4 v9, s[36:37] offset:1024\n\t"
    "v_exp_f32 v42, v42\n\t"
    "v_exp_f32 v43, v43\n\t"
    "v_exp_f32 v44, v44\n\t"
    "v_exp_f32 v45, v45\n\t"
    "v_mfma_f32_32x32x16_bf16 v[168:183], v[68:71], v[240:243], v[168:183]\n\t"
    "ds_read_b64_tr_b16 v[208:209], v10 offset:16384\n\t"
    "v_exp_f32 v46, v46\n\t"
    "v_exp_f32 v47, v47\n\t"
    "v_exp_f32 v48, v48\n\t"
    "s_waitcnt lgkmcnt(1)\n\t"
    "v_exp_f32 v49, v49\n\t"
    "v_smfmac_f32_32x32x32_bf16 v[16:31], v[112:115], v[40:47], v15\n\t"
    "ds_read_b64_tr_b16 v[210:211], v10 offset:16640\n\t"
    "ds_read_b64_tr_b16 v[212:213], v10 offset:16896\n\t"
    "v_exp_f32 v50, v50\n\t"
    ".p2align 12\n\t"
    ".Lfmm_page5:\n\t"
    "s_cbranch_execz .Lfmm_ret\n\t"
    "v_exp_f32 v51, v51\n\t"
    "v_exp_f32 v52, v52\n\t"
    "v_exp_f32 v53, v53\n\t"
    "v_mfma_f32_32x32x16_bf16 v[32:47], v[100:103], v[104:107], -4.0\n\t"
    "ds_read_b64_tr_b16 v[214:215], v10 offset:17152\n\t"
    "v_exp_f32 v54, v54\n\t"
    "s_add_u32 s36, s36, 0x4000\n\t"
    "v_exp_f32 v55, v55\n\t"
    "s_addc_u32 s37, s37, 0\n\t"
    "v_exp_f32 v56, v56\n\t"
    "v_exp_f32 v57, v57\n\t"
    "v_smfmac_f32_32x32x32_bf16 v[116:131], v[108:111], v[48:55], v15\n\t"
    "v_exp_f32 v58, v58\n\t"
    "s_add_u32 m0, s46, 147456\n\t"
    "v_exp_f32 v59, v59\n\t"
    "v_exp_f32 v60, v60\n\t"
    "v_exp_f32 v61, v61\n\t"
    "v_mfma_f32_32x32x16_bf16 v[184:199], v[68:71], v[244:247], v[184:199]\n\t"
    "v_exp_f32 v62, v62\n\t"
    "v_exp_f32 v63, v63\n\t"
    "v_exp_f32 v32, v32\n\t"
    "s_waitcnt lgkmcnt(0)\n\t"
    "v_exp_f32 v33, v33\n\t"
    "v_smfmac_f32_32x32x32_bf16 v[116:131], v[112:115], v[56:63], v15\n\t"
    "v_add_u32 v5, 65536, v5\n\t"
    "s_mov_b64 exec, s[66:67]\n\t"
    "ds_read_b128 v[108:111], v5 offset:0\n\t"
    "ds_read_b128 v[112:115], v5 offset:512\n\t"
    "s_mov_b64 exec, -1\n\t"
    "v_exp_f32 v34, v34\n\t"
    "v_exp_f32 v35, v35\n\t"
    "v_exp_f32 v36, v36\n\t"
    "v_exp_f32 v37, v37\n\t"
    "v_mfma_f32_32x32x16_bf16 v[48:63], v[100:103], v[96:99], -4.0\n\t"
    "ds_read_b128 v[100:103], v14 offset:4096\n\t"
    "global_load_lds_dwordx4 v8, s[36:37]\n\t"
    "v_exp_f32 v38, v38\n\t"
    "v_exp_f32 v39, v39\n\t"
    "v_exp_f32 v40, v40\n\t"
    "s_waitcnt lgkmcnt(2)\n\t"
    "v_exp_f32 v41, v41\n\t"
    "v_smfmac_f32_32x32x32_bf16 v[16:31], v[108:111], v[32:39], v15\n\t"
    "global_load_lds_dwordx4 v9, s[36:37] offset:1024\n\t"
    "global_load_dwordx4 v[240:243], v4, s[52:53] offset:2048 sc1\n\t"
    "v_exp_f32 v42, v42\n\t"
    "v_exp_f32 v43, v43\n\t"
    "v_exp_f32 v44, v44\n\t"
    "v_exp_f32 v45, v45\n\t"
    "v_mfma_f32_32x32x16_bf16 v[136:151], v[72:75], v[248:251], v[136:151]\n\t"
    "global_load_dwordx4 v[244:247], v4, s[52:53] offset:3072 sc1\n\t"
    "v_exp_f32 v46, v46\n\t"
    "v_exp_f32 v47, v47\n\t"
    "v_exp_f32 v48, v48\n\t"
    "s_waitcnt lgkmcnt(0)\n\t"
    "v_exp_f32 v49, v49\n\t"
    "v_smfmac_f32_32x32x32_bf16 v[16:31], v[112:115], v[40:47], v15\n\t"
    "v_exp_f32 v50, v50\n\t"
    "v_exp_f32 v51, v51\n\t"
    "v_exp_f32 v52, v52\n\t"
    "v_exp_f32 v53, v53\n\t"
    "v_mfma_f32_32x32x16_bf16 v[32:47], v[100:103], v[104:107], -4.0\n\t"
    "v_exp_f32 v54, v54\n\t"
    "v_exp_f32 v55, v55\n\t"
    "v_exp_f32 v56, v56\n\t"
    "s_waitcnt vmcnt(4)\n\t"
    "v_exp_f32 v57, v57\n\t"
    "v_smfmac_f32_32x32x32_bf16 v[116:131], v[108:111], v[48:55], v15\n\t"
    "v_exp_f32 v58, v58\n\t"
    "v_exp_f32 v59, v59\n\t"
    "v_exp_f32 v60, v60\n\t"
    "v_exp_f32 v61, v61\n\t"
    "v_mfma_f32_32x32x16_bf16 v[152:167], v[72:75], v[252:255], v[152:167]\n\t"
    "ds_read_b64_tr_b16 v[64:65], v10 offset:0\n\t"
    "v_exp_f32 v62, v62\n\t"
    "v_exp_f32 v63, v63\n\t"
    "v_exp_f32 v32, v32\n\t"
    "v_exp_f32 v33, v33\n\t"
    "v_smfmac_f32_32x32x32_bf16 v[116:131], v[112:115], v[56:63], v15\n\t"
    "s_mov_b64 exec, s[66:67]\n\t"
    "ds_read_b128 v[108:111], v5 offset:4096\n\t"
    "ds_read_b128 v[112:115], v5 offset:4608\n\t"
    "s_mov_b64 exec, -1\n\t"
    "v_exp_f32 v34, v34\n\t"
    "v_exp_f32 v35, v35\n\t"
    "v_exp_f32 v36, v36\n\t"
    "v_exp_f32 v37, v37\n\t"
    "v_mfma_f32_32x32x16_bf16 v[48:63], v[100:103], v[96:99], -4.0\n\t"
    "ds_read_b128 v[100:103], v14 offset:8192\n\t"
    "ds_read_b64_tr_b16 v[66:67], v10 offset:256\n\t"
    "v_exp_f32 v38, v38\n\t"
    "s_add_u32 s32, s32, 0x4000\n\t"
    "v_exp_f32 v39, v39\n\t"
    "s_addc_u32 s33, s33, 0\n\t"
    "v_exp_f32 v40, v40\n\t"
    "s_waitcnt lgkmcnt(3)\n\t"
    "v_exp_f32 v41, v41\n\t"
    "v_smfmac_f32_32x32x32_bf16 v[16:31], v[108:111], v[32:39], v15\n\t"
    "ds_read_b64_tr_b16 v[68:69], v10 offset:512\n\t"
    "ds_read_b64_tr_b16 v[70:71], v10 offset:768\n\t"
    "v_exp_f32 v42, v42\n\t"
    "s_add_u32 m0, s46, 131072\n\t"
    "v_exp_f32 v43, v43\n\t"
    "v_exp_f32 v44, v44\n\t"
    "v_exp_f32 v45, v45\n\t"
    "v_mfma_f32_32x32x16_bf16 v[168:183], v[76:79], v[248:251], v[168:183]\n\t"
    "v_exp_f32 v46, v46\n\t"
    "v_exp_f32 v47, v47\n\t"
    "v_exp_f32 v48, v48\n\t"
    "s_waitcnt lgkmcnt(0)\n\t"
    "v_exp_f32 v49, v49\n\t"
    "v_smfmac_f32_32x32x32_bf16 v[16:31], v[112:115], v[40:47], v15\n\t"
    "global_load_lds_dwordx4 v8, s[32:33]\n\t"
    "v_exp_f32 v50, v50\n\t"
    "v_exp_f32 v51, v51\n\t"
    "v_exp_f32 v52, v52\n\t"
    "v_exp_f32 v53, v53\n\t"
    "v_mfma_f32_32x32x16_bf16 v[32:47], v[100:103], v[104:107], -4.0\n\t"
    "global_load_lds_dwordx4 v9, s[32:33] offset:1024\n\t"
    "v_exp_f32 v54, v54\n\t"
    "v_exp_f32 v55, v55\n\t"
    "v_exp_f32 v56, v56\n\t"
    "v_exp_f32 v57, v57\n\t"
    "v_smfmac_f32_32x32x32_bf16 v[116:131], v[108:111], v[48:55], v15\n\t"
    "v_exp_f32 v58, v58\n\t"
    "v_exp_f32 v59, v59\n\t"
    "v_exp_f32 v60, v60\n\t"
    "v_exp_f32 v61, v61\n\t"
    "v_mfma_f32_32x32x16_bf16 v[184:199], v[76:79], v[252:255], v[184:199]\n\t"
    "v_exp_f32 v62, v62\n\t"
    "v_exp_f32 v63, v63\n\t"
    "v_exp_f32 v32, v32\n\t"
    "v_exp_f32 v33, v33\n\t"
    "v_smfmac_f32_32x32x32_bf16 v[116:131], v[112:115], v[56:63], v15\n\t"
    "s_mov_b64 exec, s[66:67]\n\t"
    "ds_read_b128 v[108:111], v5 offset:8192\n\t"
    "ds_read_b128 v[112:115], v5 offset:8704\n\t"
    "s_mov_b64 exec, -1\n\t"
    "v_exp_f32 v34, v34\n\t"
    "s_add_u32 s52, s52, 0x2000\n\t"
    "v_exp_f32 v35, v35\n\t"
    "s_addc_u32 s53, s53, 0\n\t"
    "v_exp_f32 v36, v36\n\t"
    "v_exp_f32 v37, v37\n\t"
    "v_mfma_f32_32x32x16_bf16 v[48:63], v[100:103], v[96:99], -4.0\n\t"
    "ds_read_b128 v[100:103], v14 offset:12288\n\t"
    "global_load_dwordx4 v[248:251], v4, s[52:53] offset:-4096 sc1\n\t"
    "v_exp_f32 v38, v38\n\t"
    "v_exp_f32 v39, v39\n\t"
    "v_exp_f32 v40, v40\n\t"
    "s_waitcnt vmcnt(5) lgkmcnt(2)\n\t"
    "v_exp_f32 v41, v41\n\t"
    "v_smfmac_f32_32x32x32_bf16 v[16:31], v[108:111], v[32:39], v15\n\t"
    "global_load_dwordx4 v[252:255], v4, s[52:53] offset:-3072 sc1\n\t"
    "v_exp_f32 v42, v42\n\t"
    "v_exp_f32 v43, v43\n\t"
    "v_exp_f32 v44, v44\n\t"
    "v_exp_f32 v45, v45\n\t"
    "v_mfma_f32_32x32x16_bf16 v[136:151], v[64:67], v[80:83], v[136:151]\n\t"
    "ds_read_b64_tr_b16 v[72:73], v10 offset:16384\n\t"
    "v_exp_f32 v46, v46\n\t"
    "s_setprio 2\n\t"
    "v_exp_f32 v47, v47\n\t"
    "v_exp_f32 v48, v48\n\t"
    "s_waitcnt lgkmcnt(1)\n\t"
    "v_exp_f32 v49, v49\n\t"
    "v_smfmac_f32_32x32x32_bf16 v[16:31], v[112:115], v[40:47], v15\n\t"
    "ds_read_b64_tr_b16 v[74:75], v10 offset:16640\n\t"
    "ds_read_b64_tr_b16 v[76:77], v10 offset:16896\n\t"
    "v_exp_f32 v50, v50\n\t"
    "v_exp_f32 v51, v51\n\t"
    "v_exp_f32 v52, v52\n\t"
    "v_exp_f32 v53, v53\n\t"
    "v_mfma_f32_32x32x16_bf16 v[32:47], v[100:103], v[104:107], -4.0\n\t"
    "ds_read_b64_tr_b16 v[78:79], v10 offset:17152\n\t"
    "v_exp_f32 v54, v54\n\t"
    "s_add_u32 s36, s36, 0x4000\n\t"
    "v_exp_f32 v55, v55\n\t"
    "s_addc_u32 s37, s37, 0\n\t"
    "v_exp_f32 v56, v56\n\t"
    "v_exp_f32 v57, v57\n\t"
    "v_smfmac_f32_32x32x32_bf16 v[116:131], v[108:111], v[48:55], v15\n\t"
    "v_exp_f32 v58, v58\n\t"
    "s_add_u32 m0, s46, 147456\n\t"
    "v_exp_f32 v59, v59\n\t"
    "v_exp_f32 v60, v60\n\t"
    "v_exp_f32 v61, v61\n\t"
    "v_mfma_f32_32x32x16_bf16 v[152:167], v[64:67], v[84:87], v[152:167]\n\t"
    "v_exp_f32 v62, v62\n\t"
    "v_exp_f32 v63, v63\n\t"
    "v_exp_f32 v32, v32\n\t"
    "s_waitcnt lgkmcnt(0)\n\t"
    "v_exp_f32 v33, v33\n\t"
    "v_smfmac_f32_32x32x32_bf16 v[116:131], v[112:115], v[56:63], v15\n\t"
    "s_mov_b64 exec, s[66:67]\n\t"
    "ds_read_b128 v[108:111], v5 offset:12288\n\t"
    "ds_read_b128 v[112:115], v5 offset:12800\n\t"
    "s_mov_b64 exec, -1\n\t"
    "v_exp_f32 v34, v34\n\t"
    "v_exp_f32 v35, v35\n\t"
    "v_exp_f32 v36, v36\n\t"
    "v_exp_f32 v37, v37\n\t"
    "v_mfma_f32_32x32x16_bf16 v[48:63], v[100:103], v[96:99], -4.0\n\t"
    "ds_read_b128 v[100:103], v14 offset:16384\n\t"
    "global_load_lds_dwordx4 v8, s[36:37]\n\t"
    "v_exp_f32 v38, v38\n\t"
    "v_exp_f32 v39, v39\n\t"
    "v_exp_f32 v40, v40\n\t"
    "s_waitcnt vmcnt(3) lgkmcnt(2)\n\t"
    "v_exp_f32 v41, v41\n\t"
    "v_smfmac_f32_32x32x32_bf16 v[16:31], v[108:111], v[32:39], v15\n\t"
    "global_load_lds_dwordx4 v9, s[36:37] offset:1024\n\t"
    "v_exp_f32 v42, v42\n\t"
    "v_exp_f32 v43, v43\n\t"
    "v_exp_f32 v44, v44\n\t"
    "v_exp_f32 v45, v45\n\t"
    "v_mfma_f32_32x32x16_bf16 v[168:183], v[68:71], v[80:83], v[168:183]\n\t"
    "ds_read_b64_tr_b16 v[216:217], v10 offset:0\n\t"
    "v_exp_f32 v46, v46\n\t"
    "v_exp_f32 v47, v47\n\t"
    "v_exp_f32 v48, v48\n\t"
    "s_waitcnt lgkmcnt(1)\n\t"
    "v_exp_f32 v49, v49\n\t"
    "v_smfmac_f32_32x32x32_bf16 v[16:31], v[112:115], v[40:47], v15\n\t"
    "ds_read_b64_tr_b16 v[218:219], v10 offset:256\n\t"
    "ds_read_b64_tr_b16 v[220:221], v10 offset:512\n\t"
    "v_exp_f32 v50, v50\n\t"
    "v_exp_f32 v51, v51\n\t"
    "v_exp_f32 v52, v52\n\t"
    "v_exp_f32 v53, v53\n\t"
    "v_mfma_f32_32x32x16_bf16 v[32:47], v[100:103], v[104:107], -4.0\n\t"
    "ds_read_b64_tr_b16 v[222:223], v10 offset:768\n\t"
    "v_exp_f32 v54, v54\n\t"
    "s_add_u32 s36, s36, 0x4000\n\t"
    "v_exp_f32 v55, v55\n\t"
    "s_addc_u32 s37, s37, 0\n\t"
    "v_exp_f32 v56, v56\n\t"
    "v_exp_f32 v57, v57\n\t"
    "v_smfmac_f32_32x32x32_bf16 v[116:131], v[108:111], v[48:55], v15\n\t"
    "v_exp_f32 v58, v58\n\t"
    "s_add_u32 m0, s46, 131072\n\t"
    "v_exp_f32 v59, v59\n\t"
    "v_exp_f32 v60, v60\n\t"
    "v_exp_f32 v61, v61\n\t"
    "v_mfma_f32_32x32x16_bf16 v[184:199], v[68:71], v[84:87], v[184:199]\n\t"
    "v_exp_f32 v62, v62\n\t"
    "v_exp_f32 v63, v63\n\t"
    "v_exp_f32 v32, v32\n\t"
    "s_waitcnt lgkmcnt(0)\n\t"
    "v_exp_f32 v33, v33\n\t"
    "v_smfmac_f32_32x32x32_bf16 v[116:131], v[112:115], v[56:63], v15\n\t"
    "s_mov_b64 exec, s[66:67]\n\t"
    "ds_read_b128 v[108:111], v5 offset:16384\n\t"
    "ds_read_b128 v[112:115], v5 offset:16896\n\t"
    "s_mov_b64 exec, -1\n\t"
    "v_exp_f32 v34, v34\n\t"
    "v_exp_f32 v35, v35\n\t"
    "v_exp_f32 v36, v36\n\t"
    "v_exp_f32 v37, v37\n\t"
    "v_mfma_f32_32x32x16_bf16 v[48:63], v[100:103], v[96:99], -4.0\n\t"
    "ds_read_b128 v[100:103], v14 offset:20480\n\t"
    "global_load_lds_dwordx4 v8, s[36:37]\n\t"
    "v_exp_f32 v38, v38\n\t"
    "v_exp_f32 v39, v39\n\t"
    "v_exp_f32 v40, v40\n\t"
    "s_waitcnt lgkmcnt(2)\n\t"
    "v_exp_f32 v41, v41\n\t"
    "v_smfmac_f32_32x32x32_bf16 v[16:31], v[108:111], v[32:39], v15\n\t"
    "global_load_lds_dwordx4 v9, s[36:37] offset:1024\n\t"
    "global_load_dwordx4 v[80:83], v4, s[52:53] offset:-2048 sc1\n\t"
    "v_exp_f32 v42, v42\n\t"
    "v_exp_f32 v43, v43\n\t"
    "v_exp_f32 v44, v44\n\t"
    "v_exp_f32 v45, v45\n\t"
    "v_mfma_f32_32x32x16_bf16 v[136:151], v[72:75], v[88:91], v[136:151]\n\t"
    "global_load_dwordx4 v[84:87], v4, s[52:53] offset:-1024 sc1\n\t"
    "v_exp_f32 v46, v46\n\t"
    "v_exp_f32 v47, v47\n\t"
    "v_exp_f32 v48, v48\n\t"
    "s_waitcnt lgkmcnt(0)\n\t"
    "v_exp_f32 v49, v49\n\t"
    "v_smfmac_f32_32x32x32_bf16 v[16:31], v[112:115], v[40:47], v15\n\t"
    "v_exp_f32 v50, v50\n\t"
    "v_exp_f32 v51, v51\n\t"
    "v_exp_f32 v52, v52\n\t"
    "v_exp_f32 v53, v53\n\t"
    "v_mfma_f32_32x32x16_bf16 v[32:47], v[100:103], v[104:107], -4.0\n\t"
    "v_exp_f32 v54, v54\n\t"
    "v_exp_f32 v55, v55\n\t"
    "v_exp_f32 v56, v56\n\t"
    "s_waitcnt vmcnt(4)\n\t"
    "v_exp_f32 v57, v57\n\t"
    "v_smfmac_f32_32x32x32_bf16 v[116:131], v[108:111], v[48:55], v15\n\t"
    "v_exp_f32 v58, v58\n\t"
    "v_exp_f32 v59, v59\n\t"
    "v_exp_f32 v60, v60\n\t"
    "v_exp_f32 v61, v61\n\t"
    "v_mfma_f32_32x32x16_bf16 v[152:167], v[72:75], v[92:95], v[152:167]\n\t"
    "ds_read_b64_tr_b16 v[64:65], v10 offset:16384\n\t"
    "v_exp_f32 v62, v62\n\t"
    "v_exp_f32 v63, v63\n\t"
    "v_exp_f32 v32, v32\n\t"
    "v_exp_f32 v33, v33\n\t"
    "v_smfmac_f32_32x32x32_bf16 v[116:131], v[112:115], v[56:63], v15\n\t"
    "s_mov_b64 exec, s[66:67]\n\t"
    "ds_read_b128 v[108:111], v5 offset:20480\n\t"
    "ds_read_b128 v[112:115], v5 offset:20992\n\t"
    "s_mov_b64 exec, -1\n\t"
    "v_exp_f32 v34, v34\n\t"
    "v_exp_f32 v35, v35\n\t"
    "v_exp_f32 v36, v36\n\t"
    "v_exp_f32 v37, v37\n\t"
    "v_mfma_f32_32x32x16_bf16 v[48:63], v[100:103], v[96:99], -4.0\n\t"
    "ds_read_b128 v[100:103], v14 offset:24576\n\t"
    "ds_read_b64_tr_b16 v[66:67], v10 offset:16640\n\t"
    "v_exp_f32 v38, v38\n\t"
    "s_add_u32 s32, s32, 0x4000\n\t"
    "v_exp_f32 v39, v39\n\t"
    "s_addc_u32 s33, s33, 0\n\t"
    "v_exp_f32 v40, v40\n\t"
    "s_waitcnt lgkmcnt(3)\n\t"
    "v_exp_f32 v41, v41\n\t"
    "v_smfmac_f32_32x32x32_bf16 v[16:31], v[108:111], v[32:39], v15\n\t"
    "ds_read_b64_tr_b16 v[68:69], v10 offset:16896\n\t"
    "ds_read_b64_tr_b16 v[70:71], v10 offset:17152\n\t"
    "v_exp_f32 v42, v42\n\t"
    "s_add_u32 m0, s46, 147456\n\t"
    "v_exp_f32 v43, v43\n\t"
    "v_exp_f32 v44, v44\n\t"
    "v_exp_f32 v45, v45\n\t"
    "v_mfma_f32_32x32x16_bf16 v[168:183], v[76:79], v[88:91], v[168:183]\n\t"
    "v_exp_f32 v46, v46\n\t"
    "v_exp_f32 v47, v47\n\t"
    "v_exp_f32 v48, v48\n\t"
    "s_waitcnt lgkmcnt(0)\n\t"
    "v_exp_f32 v49, v49\n\t"
    "v_smfmac_f32_32x32x32_bf16 v[16:31], v[112:115], v[40:47], v15\n\t"
    "global_load_lds_dwordx4 v8, s[32:33]\n\t"
    "v_exp_f32 v50, v50\n\t"
    "v_exp_f32 v51, v51\n\t"
    "v_exp_f32 v52, v52\n\t"
    "v_exp_f32 v53, v53\n\t"
    "v_mfma_f32_32x32x16_bf16 v[32:47], v[100:103], v[104:107], -4.0\n\t"
    "global_load_lds_dwordx4 v9, s[32:33] offset:1024\n\t"
    "v_exp_f32 v54, v54\n\t"
    "v_exp_f32 v55, v55\n\t"
    "v_exp_f32 v56, v56\n\t"
    "v_exp_f32 v57, v57\n\t"
    "v_smfmac_f32_32x32x32_bf16 v[116:131], v[108:111], v[48:55], v15\n\t"
    "v_exp_f32 v58, v58\n\t"
    "v_exp_f32 v59, v59\n\t"
    "v_exp_f32 v60, v60\n\t"
    "v_exp_f32 v61, v61\n\t"
    "v_mfma_f32_32x32x16_bf16 v[184:199], v[76:79], v[92:95], v[184:199]\n\t"
    "v_exp_f32 v62, v62\n\t"
    "v_exp_f32 v63, v63\n\t"
    "v_exp_f32 v32, v32\n\t"
    "v_exp_f32 v33, v33\n\t"
    "v_smfmac_f32_32x32x32_bf16 v[116:131], v[112:115], v[56:63], v15\n\t"
    "s_mov_b64 exec, s[66:67]\n\t"
    "ds_read_b128 v[108:111], v5 offset:24576\n\t"
    "ds_read_b128 v[112:115], v5 offset:25088\n\t"
    "s_mov_b64 exec, -1\n\t"
    "v_exp_f32 v34, v34\n\t"
    "v_exp_f32 v35, v35\n\t"
    "v_exp_f32 v36, v36\n\t"
    "v_exp_f32 v37, v37\n\t"
    "v_mfma_f32_32x32x16_bf16 v[48:63], v[100:103], v[96:99], -4.0\n\t"
    "ds_read_b128 v[100:103], v14 offset:28672\n\t"
    "global_load_dwordx4 v[88:91], v4, s[52:53] offset:0 sc1\n\t"
    "v_exp_f32 v38, v38\n\t"
    "v_exp_f32 v39, v39\n\t"
    "v_exp_f32 v40, v40\n\t"
    "s_waitcnt vmcnt(5) lgkmcnt(2)\n\t"
    "v_exp_f32 v41, v41\n\t"
    "v_smfmac_f32_32x32x32_bf16 v[16:31], v[108:111], v[32:39], v15\n\t"
    "global_load_dwordx4 v[92:95], v4, s[52:53] offset:1024 sc1\n\t"
    "v_exp_f32 v42, v42\n\t"
    "v_exp_f32 v43, v43\n\t"
    "v_exp_f32 v44, v44\n\t"
    "v_exp_f32 v45, v45\n\t"
    "v_mfma_f32_32x32x16_bf16 v[136:151], v[64:67], v[232:235], v[136:151]\n\t"
    "ds_read_b64_tr_b16 v[72:73], v10 offset:0\n\t"
    "v_exp_f32 v46, v46\n\t"
    "v_exp_f32 v47, v47\n\t"
    "v_exp_f32 v48, v48\n\t"
    "s_waitcnt lgkmcnt(1)\n\t"
    "v_exp_f32 v49, v49\n\t"
    "v_smfmac_f32_32x32x32_bf16 v[16:31], v[112:115], v[40:47], v15\n\t"
    "ds_read_b64_tr_b16 v[74:75], v10 offset:256\n\t"
    "ds_read_b64_tr_b16 v[76:77], v10 offset:512\n\t"
    "v_exp_f32 v50, v50\n\t"
    "v_exp_f32 v51, v51\n\t"
    "v_exp_f32 v52, v52\n\t"
    "v_exp_f32 v53, v53\n\t"
    "v_mfma_f32_32x32x16_bf16 v[32:47], v[100:103], v[104:107], -4.0\n\t"
    "ds_read_b64_tr_b16 v[78:79], v10 offset:768\n\t"
    "v_exp_f32 v54, v54\n\t"
    "s_add_u32 s36, s36, 0x4000\n\t"
    "v_exp_f32 v55, v55\n\t"
    "s_addc_u32 s37, s37, 0\n\t"
    "v_exp_f32 v56, v56\n\t"
    "v_exp_f32 v57, v57\n\t"
    "v_smfmac_f32_32x32x32_bf16 v[116:131], v[108:111], v[48:55], v15\n\t"
    "v_exp_f32 v58, v58\n\t"
    "s_add_u32 m0, s46, 131072\n\t"
    "v_exp_f32 v59, v59\n\t"
    "v_exp_f32 v60, v60\n\t"
    "v_exp_f32 v61, v61\n\t"
    "v_mfma_f32_32x32x16_bf16 v[152:167], v[64:67], v[236:239], v[152:167]\n\t"
    "v_exp_f32 v62, v62\n\t"
    "v_exp_f32 v63, v63\n\t"
    "v_exp_f32 v32, v32\n\t"
    "s_waitcnt lgkmcnt(0)\n\t"
    "v_exp_f32 v33, v33\n\t"
    "v_smfmac_f32_32x32x32_bf16 v[116:131], v[112:115], v[56:63], v15\n\t"
    "s_mov_b64 exec, s[66:67]\n\t"
    "ds_read_b128 v[108:111], v5 offset:28672\n\t"
    "ds_read_b128 v[112:115], v5 offset:29184\n\t"
    "s_mov_b64 exec, -1\n\t"
    "v_exp_f32 v34, v34\n\t"
    "v_exp_f32 v35, v35\n\t"
    "v_exp_f32 v36, v36\n\t"
    "v_exp_f32 v37, v37\n\t"
    "v_mfma_f32_32x32x16_bf16 v[48:63], v[100:103], v[96:99], -4.0\n\t"
    "v_add_u32 v14, 4294868992, v14\n\t"
    "ds_read_b128 v[100:103], v14 offset:0\n\t"
    "global_load_lds_dwordx4 v8, s[36:37]\n\t"
    "v_exp_f32 v38, v38\n\t"
    "v_exp_f32 v39, v39\n\t"
    "v_exp_f32 v40, v40\n\t"
    "s_waitcnt vmcnt(3) lgkmcnt(2)\n\t"
    "v_exp_f32 v41, v41\n\t"
    "v_smfmac_f32_32x32x32_bf16 v[16:31], v[108:111], v[32:39], v15\n\t"
    "global_load_lds_dwordx4 v9, s[36:37] offset:1024\n\t"
    "v_exp_f32 v42, v42\n\t"
    "v_exp_f32 v43, v43\n\t"
    "v_exp_f32 v44, v44\n\t"
    "v_exp_f32 v45, v45\n\t"
    "v_mfma_f32_32x32x16_bf16 v[168:183], v[68:71], v[232:235], v[168:183]\n\t"
    "ds_read_b64_tr_b16 v[224:225], v10 offset:16384\n\t"
    "v_exp_f32 v46, v46\n\t"
    "v_exp_f32 v47, v47\n\t"
    "v_exp_f32 v48, v48\n\t"
    "s_waitcnt lgkmcnt(1)\n\t"
    "v_exp_f32 v49, v49\n\t"
    "v_smfmac_f32_32x32x32_bf16 v[16:31], v[112:115], v[40:47], v15\n\t"
    "ds_read_b64_tr_b16 v[226:227], v10 offset:16640\n\t"
    "ds_read_b64_tr_b16 v[228:229], v10 offset:16896\n\t"
    "v_exp_f32 v50, v50\n\t"
    "v_exp_f32 v51, v51\n\t"
    "v_exp_f32 v52, v52\n\t"
    "v_exp_f32 v53, v53\n\t"
    "v_mfma_f32_32x32x16_bf16 v[32:47], v[100:103], v[104:107], -4.0\n\t"
    "ds_read_b64_tr_b16 v[230:231], v10 offset:17152\n\t"
    "v_exp_f32 v54, v54\n\t"
    "s_add_u32 s36, s36, 0x4000\n\t"
    "v_exp_f32 v55, v55\n\t"
    "s_addc_u32 s37, s37, 0\n\t"
    "v_exp_f32 v56, v56\n\t"
    "v_exp_f32 v57, v57\n\t"
    "v_smfmac_f32_32x32x32_bf16 v[116:131], v[108:111], v[48:55], v15\n\t"
    "v_exp_f32 v58, v58\n\t"
    "s_add_u32 m0, s46, 147456\n\t"
    "v_exp_f32 v59, v59\n\t"
    "v_exp_f32 v60, v60\n\t"
    "v_exp_f32 v61, v61\n\t"
    "v_mfma_f32_32x32x16_bf16 v[184:199], v[68:71], v[236:239], v[184:199]\n\t"
    "v_exp_f32 v62, v62\n\t"
    "v_exp_f32 v63, v63\n\t"
    "v_exp_f32 v32, v32\n\t"
    "s_waitcnt lgkmcnt(0)\n\t"
    "v_exp_f32 v33, v33\n\t"
    "v_smfmac_f32_32x32x32_bf16 v[116:131], v[112:115], v[56:63], v15\n\t"
    "v_add_u32 v5, 4294868992, v5\n\t"
    "s_mov_b64 exec, s[66:67]\n\t"
    "ds_read_b128 v[108:111], v5 offset:0\n\t"
    "ds_read_b128 v[112:115], v5 offset:512\n\t"
    "s_mov_b64 exec, -1\n\t"
    "v_exp_f32 v34, v34\n\t"
    "v_exp_f32 v35, v35\n\t"
    "v_exp_f32 v36, v36\n\t"
    "v_exp_f32 v37, v37\n\t"
    "v_mfma_f32_32x32x16_bf16 v[48:63], v[100:103], v[96:99], -4.0\n\t"
    "ds_read_b128 v[100:103], v14 offset:4096\n\t"
    "global_load_lds_dwordx4 v8, s[36:37]\n\t"
    "v_exp_f32 v38, v38\n\t"
    "v_exp_f32 v39, v39\n\t"
    "v_exp_f32 v40, v40\n\t"
    "s_waitcnt lgkmcnt(2)\n\t"
    "v_exp_f32 v41, v41\n\t"
    "v_smfmac_f32_32x32x32_bf16 v[16:31], v[108:111], v[32:39], v15\n\t"
    "global_load_lds_dwordx4 v9, s[36:37] offset:1024\n\t"
    "global_load_dwordx4 v[232:235], v4, s[52:53] offset:2048 sc1\n\t"
    "v_exp_f32 v42, v42\n\t"
    "v_exp_f32 v43, v43\n\t"
    "v_exp_f32 v44, v44\n\t"
    "v_exp_f32 v45, v45\n\t"
    "v_mfma_f32_32x32x16_bf16 v[136:151], v[72:75], v[240:243], v[136:151]\n\t"
    "global_load_dwordx4 v[236:239], v4, s[52:53] offset:3072 sc1\n\t"
    "v_exp_f32 v46, v46\n\t"
    "v_exp_f32 v47, v47\n\t"
    "v_exp_f32 v48, v48\n\t"
    "s_waitcnt lgkmcnt(0)\n\t"
    "v_exp_f32 v49, v49\n\t"
    "v_smfmac_f32_32x32x32_bf16 v[16:31], v[112:115], v[40:47], v15\n\t"
    "v_exp_f32 v50, v50\n\t"
    "v_exp_f32 v51, v51\n\t"
    "v_exp_f32 v52, v52\n\t"
    "v_exp_f32 v53, v53\n\t"
    "v_mfma_f32_32x32x16_bf16 v[32:47], v[100:103], v[104:107], -4.0\n\t"
    "v_exp_f32 v54, v54\n\t"
    "v_exp_f32 v55, v55\n\t"
    "v_exp_f32 v56, v56\n\t"
    "s_waitcnt vmcnt(4)\n\t"
    "v_exp_f32 v57, v57\n\t"
    "v_smfmac_f32_32x32x32_bf16 v[116:131], v[108:111], v[48:55], v15\n\t"
    "v_exp_f32 v58, v58\n\t"
    "v_exp_f32 v59, v59\n\t"
    "v_exp_f32 v60, v60\n\t"
    "v_exp_f32 v61, v61\n\t"
    "v_mfma_f32_32x32x16_bf16 v[152:167], v[72:75], v[244:247], v[152:167]\n\t"
    "ds_read_b64_tr_b16 v[64:65], v10 offset:0\n\t"
    "v_exp_f32 v62, v62\n\t"
    "v_exp_f32 v63, v63\n\t"
    "v_exp_f32 v32, v32\n\t"
    "v_exp_f32 v33, v33\n\t"
    "v_smfmac_f32_32x32x32_bf16 v[116:131], v[112:115], v[56:63], v15\n\t"
    "s_mov_b64 exec, s[66:67]\n\t"
    "ds_read_b128 v[108:111], v5 offset:4096\n\t"
    "ds_read_b128 v[112:115], v5 offset:4608\n\t"
    "s_mov_b64 exec, -1\n\t"
    "v_exp_f32 v34, v34\n\t"
    "v_exp_f32 v35, v35\n\t"
    "v_exp_f32 v36, v36\n\t"
    "v_exp_f32 v37, v37\n\t"
    "v_mfma_f32_32x32x16_bf16 v[48:63], v[100:103], v[96:99], -4.0\n\t"
    "ds_read_b128 v[100:103], v14 offset:8192\n\t"
    "ds_read_b64_tr_b16 v[66:67], v10 offset:256\n\t"
    "v_exp_f32 v38, v38\n\t"
    "s_add_u32 s36, s36, 0x4000\n\t"
    "v_exp_f32 v39, v39\n\t"
    "s_addc_u32 s37, s37, 0\n\t"
    "v_exp_f32 v40, v40\n\t"
    "s_waitcnt lgkmcnt(3)\n\t"
    "v_exp_f32 v41, v41\n\t"
    "v_smfmac_f32_32x32x32_bf16 v[16:31], v[108:111], v[32:39], v15\n\t"
    "ds_read_b64_tr_b16 v[68:69], v10 offset:512\n\t"
    "ds_read_b64_tr_b16 v[70:71], v10 offset:768\n\t"
    "v_exp_f32 v42, v42\n\t"
    "s_add_u32 m0, s46, 131072\n\t"
    "v_exp_f32 v43, v43\n\t"
    "v_exp_f32 v44, v44\n\t"
    "v_exp_f32 v45, v45\n\t"
    "v_mfma_f32_32x32x16_bf16 v[168:183], v[76:79], v[240:243], v[168:183]\n\t"
    "v_exp_f32 v46, v46\n\t"
    "v_exp_f32 v47, v47\n\t"
    "v_exp_f32 v48, v48\n\t"
    "s_waitcnt lgkmcnt(0)\n\t"
    "v_exp_f32 v49, v49\n\t"
    "v_smfmac_f32_32x32x32_bf16 v[16:31], v[112:115], v[40:47], v15\n\t"
    "global_load_lds_dwordx4 v8, s[36:37]\n\t"
    "v_exp_f32 v50, v50\n\t"
    "v_exp_f32 v51, v51\n\t"
    "v_exp_f32 v52, v52\n\t"
    "v_exp_f32 v53, v53\n\t"
    "v_mfma_f32_32x32x16_bf16 v[32:47], v[100:103], v[104:107], -4.0\n\t"
    "global_load_lds_dwordx4 v9, s[36:37] offset:1024\n\t"
    "v_exp_f32 v54, v54\n\t"
    "v_exp_f32 v55, v55\n\t"
    "v_exp_f32 v56, v56\n\t"
    "v_exp_f32 v57, v57\n\t"
    "v_smfmac_f32_32x32x32_bf16 v[116:131], v[108:111], v[48:55], v15\n\t"
    "v_exp_f32 v58, v58\n\t"
    "v_exp_f32 v59, v59\n\t"
    "v_exp_f32 v60, v60\n\t"
    "v_exp_f32 v61, v61\n\t"
    "v_mfma_f32_32x32x16_bf16 v[184:199], v[76:79], v[244:247], v[184:199]\n\t"
    "v_exp_f32 v62, v62\n\t"
    "v_exp_f32 v63, v63\n\t"
    "v_exp_f32 v32, v32\n\t"
    "v_exp_f32 v33, v33\n\t"
    "v_smfmac_f32_32x32x32_bf16 v[116:131], v[112:115], v[56:63], v15\n\t"
    "s_mov_b64 exec, s[66:67]\n\t"
    "ds_read_b128 v[108:111], v5 offset:8192\n\t"
    "ds_read_b128 v[112:115], v5 offset:8704\n\t"
    "s_mov_b64 exec, -1\n\t"
    "v_exp_f32 v34, v34\n\t"
    "s_add_u32 s52, s52, 0x2000\n\t"
    "v_exp_f32 v35, v35\n\t"
    "s_addc_u32 s53, s53, 0\n\t"
    "v_exp_f32 v36, v36\n\t"
    "v_exp_f32 v37, v37\n\t"
    "v_mfma_f32_32x32x16_bf16 v[48:63], v[100:103], v[96:99], -4.0\n\t"
    "ds_read_b128 v[100:103], v14 offset:12288\n\t"
    "global_load_dwordx4 v[240:243], v4, s[52:53] offset:-4096 sc1\n\t"
    "v_exp_f32 v38, v38\n\t"
    "v_exp_f32 v39, v39\n\t"
    "v_exp_f32 v40, v40\n\t"
    "s_waitcnt vmcnt(5) lgkmcnt(2)\n\t"
    "v_exp_f32 v41, v41\n\t"
    "v_smfmac_f32_32x32x32_bf16 v[16:31], v[108:111], v[32:39], v15\n\t"
    "global_load_dwordx4 v[244:247], v4, s[52:53] offset:-3072 sc1\n\t"
    "v_exp_f32 v42, v42\n\t"
    "v_exp_f32 v43, v43\n\t"
    "v_exp_f32 v44, v44\n\t"
    "v_exp_f32 v45, v45\n\t"
    "v_mfma_f32_32x32x16_bf16 v[136:151], v[64:67], v[248:251], v[136:151]\n\t"
    "ds_read_b64_tr_b16 v[72:73], v10 offset:16384\n\t"
    "v_exp_f32 v46, v46\n\t"
    "v_exp_f32 v47, v47\n\t"
    "v_exp_f32 v48, v48\n\t"
    "s_waitcnt lgkmcnt(1)\n\t"
    "v_exp_f32 v49, v49\n\t"
    "v_smfmac_f32_32x32x32_bf16 v[16:31], v[112:115], v[40:47], v15\n\t"
    "ds_read_b64_tr_b16 v[74:75], v10 offset:16640\n\t"
    "ds_read_b64_tr_b16 v[76:77], v10 offset:16896\n\t"
    "v_exp_f32 v50, v50\n\t"
    "v_exp_f32 v51, v51\n\t"
    "v_exp_f32 v52, v52\n\t"
    "v_exp_f32 v53, v53\n\t"
    "v_mfma_f32_32x32x16_bf16 v[32:47], v[100:103], v[104:107], -4.0\n\t"
    "ds_read_b64_tr_b16 v[78:79], v10 offset:17152\n\t"
    "v_exp_f32 v54, v54\n\t"
    "s_add_u32 s36, s36, 0x4000\n\t"
    "v_exp_f32 v55, v55\n\t"
    "s_addc_u32 s37, s37, 0\n\t"
    "v_exp_f32 v56, v56\n\t"
    "v_exp_f32 v57, v57\n\t"
    "v_smfmac_f32_32x32x32_bf16 v[116:131], v[108:111], v[48:55], v15\n\t"
    "v_exp_f32 v58, v58\n\t"
    "s_add_u32 m0, s46, 147456\n\t"
    "v_exp_f32 v59, v59\n\t"
    "v_exp_f32 v60, v60\n\t"
    "v_exp_f32 v61, v61\n\t"
    "v_mfma_f32_32x32x16_bf16 v[152:167], v[64:67], v[252:255], v[152:167]\n\t"
    "v_exp_f32 v62, v62\n\t"
    "v_exp_f32 v63, v63\n\t"
    "v_exp_f32 v32, v32\n\t"
    "s_waitcnt lgkmcnt(0)\n\t"
    "v_exp_f32 v33, v33\n\t"
    "v_smfmac_f32_32x32x32_bf16 v[116:131], v[112:115], v[56:63], v15\n\t"
    "s_mov_b64 exec, s[66:67]\n\t"
    "ds_read_b128 v[108:111], v5 offset:12288\n\t"
    "ds_read_b128 v[112:115], v5 offset:12800\n\t"
    "s_mov_b64 exec, -1\n\t"
    "v_exp_f32 v34, v34\n\t"
    "v_exp_f32 v35, v35\n\t"
    "v_exp_f32 v36, v36\n\t"
    "v_exp_f32 v37, v37\n\t"
    "v_mfma_f32_32x32x16_bf16 v[48:63], v[100:103], v[96:99], -4.0\n\t"
    "ds_read_b128 v[100:103], v14 offset:16384\n\t"
    "global_load_lds_dwordx4 v8, s[36:37]\n\t"
    "v_exp_f32 v38, v38\n\t"
    "v_exp_f32 v39, v39\n\t"
    "v_exp_f32 v40, v40\n\t"
    "s_waitcnt lgkmcnt(2)\n\t"
    "v_exp_f32 v41, v41\n\t"
    "v_smfmac_f32_32x32x32_bf16 v[16:31], v[108:111], v[32:39], v15\n\t"
    "global_load_lds_dwordx4 v9, s[36:37] offset:1024\n\t"
    "v_exp_f32 v42, v42\n\t"
    "v_exp_f32 v43, v43\n\t"
    "v_exp_f32 v44, v44\n\t"
    "v_exp_f32 v45, v45\n\t"
    "v_mfma_f32_32x32x16_bf16 v[168:183], v[68:71], v[248:251], v[168:183]\n\t"
    "v_exp_f32 v46, v46\n\t"
    "v_exp_f32 v47, v47\n\t"
    "v_exp_f32 v48, v48\n\t"
    "s_waitcnt lgkmcnt(0)\n\t"
    "v_exp_f32 v49, v49\n\t"
    "v_smfmac_f32_32x32x32_bf16 v[16:31], v[112:115], v[40:47], v15\n\t"
    "v_exp_f32 v50, v50\n\t"
    "v_exp_f32 v51, v51\n\t"
    "v_exp_f32 v52, v52\n\t"
    "v_exp_f32 v53, v53\n\t"
    "v_mfma_f32_32x32x16_bf16 v[32:47], v[100:103], v[104:107], -4.0\n\t"
    "v_exp_f32 v54, v54\n\t"
    "v_exp_f32 v55, v55\n\t"
    "v_exp_f32 v56, v56\n\t"
    "s_waitcnt vmcnt(4)\n\t"
    "v_exp_f32 v57, v57\n\t"
    "v_smfmac_f32_32x32x32_bf16 v[116:131], v[108:111], v[48:55], v15\n\t"
    "v_exp_f32 v58, v58\n\t"
    "v_exp_f32 v59, v59\n\t"
    "v_exp_f32 v60, v60\n\t"
    "v_exp_f32 v61, v61\n\t"
    "v_mfma_f32_32x32x16_bf16 v[184:199], v[68:71], v[252:255], v[184:199]\n\t"
    "ds_read_b64_tr_b16 v[64:65], v10 offset:0\n\t"
    "v_exp_f32 v62, v62\n\t"
    "v_exp_f32 v63, v63\n\t"
    "v_exp_f32 v32, v32\n\t"
    "v_exp_f32 v33, v33\n\t"
    "v_smfmac_f32_32x32x32_bf16 v[116:131], v[112:115], v[56:63], v15\n\t"
    "s_mov_b64 exec, s[66:67]\n\t"
    "ds_read_b128 v[108:111], v5 offset:16384\n\t"
    "ds_read_b128 v[112:115], v5 offset:16896\n\t"
    "s_mov_b64 exec, -1\n\t"
    "v_exp_f32 v34, v34\n\t"
    "v_exp_f32 v35, v35\n\t"
    "v_exp_f32 v36, v36\n\t"
    "v_exp_f32 v37, v37\n\t"
    "v_mfma_f32_32x32x16_bf16 v[48:63], v[100:103], v[96:99], -4.0\n\t"
    "ds_read_b128 v[100:103], v14 offset:20480\n\t"
    "ds_read_b64_tr_b16 v[66:67], v10 offset:256\n\t"
    "v_exp_f32 v38, v38\n\t"
    "s_add_u32 s36, s36, 0x4000\n\t"
    "v_exp_f32 v39, v39\n\t"
    "s_addc_u32 s37, s37, 0\n\t"
    "v_exp_f32 v40, v40\n\t"
    "s_waitcnt lgkmcnt(3)\n\t"
    "v_exp_f32 v41, v41\n\t"
    "v_smfmac_f32_32x32x32_bf16 v[16:31], v[108:111], v[32:39], v15\n\t"
    "ds_read_b64_tr_b16 v[68:69], v10 offset:512\n\t"
    "ds_read_b64_tr_b16 v[70:71], v10 offset:768\n\t"
    "v_exp_f32 v42, v42\n\t"
    "s_add_u32 m0, s46, 131072\n\t"
    "v_exp_f32 v43, v43\n\t"
    "v_exp_f32 v44, v44\n\t"
    "v_exp_f32 v45, v45\n\t"
    "v_mfma_f32_32x32x16_bf16 v[136:151], v[72:75], v[80:83], v[136:151]\n\t"
    "global_load_dwordx4 v[248:251], v4, s[52:53] offset:-2048 sc1\n\t"
    "v_exp_f32 v46, v46\n\t"
    "v_exp_f32 v47, v47\n\t"
    "v_exp_f32 v48, v48\n\t"
    "s_waitcnt lgkmcnt(0)\n\t"
    "v_exp_f32 v49, v49\n\t"
    "v_smfmac_f32_32x32x32_bf16 v[16:31], v[112:115], v[40:47], v15\n\t"
    "global_load_lds_dwordx4 v8, s[36:37]\n\t"
    "global_load_dwordx4 v[252:255], v4, s[52:53] offset:-1024 sc1\n\t"
    "v_exp_f32 v50, v50\n\t"
    "v_exp_f32 v51, v51\n\t"
    "v_exp_f32 v52, v52\n\t"
    "v_exp_f32 v53, v53\n\t"
    "v_mfma_f32_32x32x16_bf16 v[32:47], v[100:103], v[104:107], -4.0\n\t"
    "global_load_lds_dwordx4 v9, s[36:37] offset:1024\n\t"
    "v_exp_f32 v54, v54\n\t"
    "v_exp_f32 v55, v55\n\t"
    "v_exp_f32 v56, v56\n\t"
    "v_exp_f32 v57, v57\n\t"
    "v_smfmac_f32_32x32x32_bf16 v[116:131], v[108:111], v[48:55], v15\n\t"
    "v_exp_f32 v58, v58\n\t"
    "v_exp_f32 v59, v59\n\t"
    "v_exp_f32 v60, v60\n\t"
    "v_exp_f32 v61, v61\n\t"
    "v_mfma_f32_32x32x16_bf16 v[152:167], v[72:75], v[84:87], v[152:167]\n\t"
    "v_exp_f32 v62, v62\n\t"
    "v_exp_f32 v63, v63\n\t"
    "v_exp_f32 v32, v32\n\t"
    "v_exp_f32 v33, v33\n\t"
    "v_smfmac_f32_32x32x32_bf16 v[116:131], v[112:115], v[56:63], v15\n\t"
    "s_mov_b64 exec, s[66:67]\n\t"
    "ds_read_b128 v[108:111], v5 offset:20480\n\t"
    "ds_read_b128 v[112:115], v5 offset:20992\n\t"
    "s_mov_b64 exec, -1\n\t"
    "v_exp_f32 v34, v34\n\t"
    "v_exp_f32 v35, v35\n\t"
    "v_exp_f32 v36, v36\n\t"
    "v_exp_f32 v37, v37\n\t"
    "v_mfma_f32_32x32x16_bf16 v[48:63], v[100:103], v[96:99], -4.0\n\t"
    "ds_read_b128 v[100:103], v14 offset:24576\n\t"
    "v_exp_f32 v38, v38\n\t"
    "v_exp_f32 v39, v39\n\t"
    "v_exp_f32 v40, v40\n\t"
    "s_waitcnt lgkmcnt(2)\n\t"
    "v_exp_f32 v41, v41\n\t"
    "v_smfmac_f32_32x32x32_bf16 v[16:31], v[108:111], v[32:39], v15\n\t"
    "v_exp_f32 v42, v42\n\t"
    "v_exp_f32 v43, v43\n\t"
    "v_exp_f32 v44, v44\n\t"
    "v_exp_f32 v45, v45\n\t"
    "v_mfma_f32_32x32x16_bf16 v[168:183], v[76:79], v[80:83], v[168:183]\n\t"
    "v_exp_f32 v46, v46\n\t"
    "v_exp_f32 v47, v47\n\t"
    "v_exp_f32 v48, v48\n\t"
    "s_waitcnt lgkmcnt(0)\n\t"
    ".p2align 12\n\t"
    ".Lfmm_page6:\n\t"
    "s_cbranch_execz .Lfmm_ret\n\t"
    "v_exp_f32 v49, v49\n\t"
    "v_smfmac_f32_32x32x32_bf16 v[16:31], v[112:115], v[40:47], v15\n\t"
    "v_exp_f32 v50, v50\n\t"
    "v_exp_f32 v51, v51\n\t"
    "v_exp_f32 v52, v52\n\t"
    "v_exp_f32 v53, v53\n\t"
    "v_mfma_f32_32x32x16_bf16 v[32:47], v[100:103], v[104:107], -4.0\n\t"
    "ds_read_b128 v[100:103], v14 offset:28672\n\t"
    "v_exp_f32 v54, v54\n\t"
    "v_exp_f32 v55, v55\n\t"
    "v_exp_f32 v56, v56\n\t"
    "s_waitcnt vmcnt(4)\n\t"
    "v_exp_f32 v57, v57\n\t"
    "v_smfmac_f32_32x32x32_bf16 v[116:131], v[108:111], v[48:55], v15\n\t"
    "v_exp_f32 v58, v58\n\t"
    "v_exp_f32 v59, v59\n\t"
    "v_exp_f32 v60, v60\n\t"
    "v_exp_f32 v61, v61\n\t"
    "v_mfma_f32_32x32x16_bf16 v[184:199], v[76:79], v[84:87], v[184:199]\n\t"
    "ds_read_b64_tr_b16 v[72:73], v10 offset:16384\n\t"
    "v_exp_f32 v62, v62\n\t"
    "s_cmp_gt_u32 s44, 3\n\t"
    "v_exp_f32 v63, v63\n\t"
    "s_cbranch_scc1 6f\n\t"
    "s_setprio 1\n\t"
    "6:\n\t"
    "v_exp_f32 v32, v32\n\t"
    "s_waitcnt lgkmcnt(1)\n\t"
    "v_exp_f32 v33, v33\n\t"
    "v_smfmac_f32_32x32x32_bf16 v[116:131], v[112:115], v[56:63], v15\n\t"
    "s_mov_b64 exec, s[66:67]\n\t"
    "ds_read_b128 v[108:111], v5 offset:24576\n\t"
    "ds_read_b128 v[112:115], v5 offset:25088\n\t"
    "s_mov_b64 exec, -1\n\t"
    "v_exp_f32 v34, v34\n\t"
    "v_exp_f32 v35, v35\n\t"
    "v_exp_f32 v36, v36\n\t"
    "v_exp_f32 v37, v37\n\t"
    "v_mfma_f32_32x32x16_bf16 v[48:63], v[100:103], v[104:107], -4.0\n\t"
    "ds_read_b128 v[100:103], v14 offset:24576\n\t"
    "ds_read_b64_tr_b16 v[74:75], v10 offset:16640\n\t"
    "v_exp_f32 v38, v38\n\t"
    "s_add_u32 s36, s36, 0x4000\n\t"
    "v_exp_f32 v39, v39\n\t"
    "s_addc_u32 s37, s37, 0\n\t"
    "v_exp_f32 v40, v40\n\t"
    "s_waitcnt lgkmcnt(3)\n\t"
    "v_exp_f32 v41, v41\n\t"
    "v_smfmac_f32_32x32x32_bf16 v[16:31], v[108:111], v[32:39], v15\n\t"
    "ds_read_b64_tr_b16 v[76:77], v10 offset:16896\n\t"
    "ds_read_b64_tr_b16 v[78:79], v10 offset:17152\n\t"
    "v_exp_f32 v42, v42\n\t"
    "s_add_u32 m0, s46, 147456\n\t"
    "v_exp_f32 v43, v43\n\t"
    "v_exp_f32 v44, v44\n\t"
    "v_exp_f32 v45, v45\n\t"
    "buffer_load_dwordx2 v[104:105], v1, s[4:7], 0 offen offset:0\n\t"
    "v_exp_f32 v46, v46\n\t"
    "v_exp_f32 v47, v47\n\t"
    "v_exp_f32 v48, v48\n\t"
    "s_waitcnt lgkmcnt(0)\n\t"
    "v_exp_f32 v49, v49\n\t"
    "v_smfmac_f32_32x32x32_bf16 v[16:31], v[112:115], v[40:47], v15\n\t"
    "s_mov_b64 exec, s[66:67]\n\t"
    "ds_read_b128 v[108:111], v5 offset:28672\n\t"
    "ds_read_b128 v[112:115], v5 offset:29184\n\t"
    "s_mov_b64 exec, -1\n\t"
    "v_exp_f32 v50, v50\n\t"
    "v_exp_f32 v51, v51\n\t"
    "v_exp_f32 v52, v52\n\t"
    "v_exp_f32 v53, v53\n\t"
    "v_mfma_f32_32x32x16_bf16 v[32:47], v[100:103], v[96:99], -4.0\n\t"
    "ds_read_b128 v[100:103], v14 offset:28672\n\t"
    "global_load_lds_dwordx4 v8, s[36:37]\n\t"
    "v_exp_f32 v54, v54\n\t"
    "v_exp_f32 v55, v55\n\t"
    "v_exp_f32 v56, v56\n\t"
    "s_waitcnt lgkmcnt(2)\n\t"
    "v_exp_f32 v57, v57\n\t"
    "v_smfmac_f32_32x32x32_bf16 v[16:31], v[108:111], v[48:55], v15\n\t"
    "global_load_lds_dwordx4 v9, s[36:37] offset:1024\n\t"
    "buffer_load_dwordx2 v[106:107], v1, s[4:7], 0 offen offset:32\n\t"
    "v_exp_f32 v58, v58\n\t"
    "v_exp_f32 v59, v59\n\t"
    "v_exp_f32 v60, v60\n\t"
    "v_exp_f32 v61, v61\n\t"
    "v_exp_f32 v62, v62\n\t"
    "v_exp_f32 v63, v63\n\t"
    "v_exp_f32 v32, v32\n\t"
    "s_waitcnt lgkmcnt(0)\n\t"
    "v_exp_f32 v33, v33\n\t"
    "v_smfmac_f32_32x32x32_bf16 v[16:31], v[112:115], v[56:63], v15\n\t"
    "s_mov_b64 exec, s[66:67]\n\t"
    "ds_read_b128 v[108:111], v5 offset:24576\n\t"
    "ds_read_b128 v[112:115], v5 offset:25088\n\t"
    "s_mov_b64 exec, -1\n\t"
    "v_exp_f32 v34, v34\n\t"
    "v_exp_f32 v35, v35\n\t"
    "v_exp_f32 v36, v36\n\t"
    "v_exp_f32 v37, v37\n\t"
    "v_mfma_f32_32x32x16_bf16 v[48:63], v[100:103], v[96:99], -4.0\n\t"
    "buffer_load_dwordx4 v[132:135], v4, s[20:23], s51 offen offset:0 sc1\n\t"
    "buffer_load_dwordx4 v[80:83], v4, s[20:23], s51 offen offset:1024 sc1\n\t"
    "v_exp_f32 v38, v38\n\t"
    "v_exp_f32 v39, v39\n\t"
    "v_exp_f32 v40, v40\n\t"
    "s_waitcnt lgkmcnt(1)\n\t"
    "v_exp_f32 v41, v41\n\t"
    "v_smfmac_f32_32x32x32_bf16 v[116:131], v[108:111], v[32:39], v15\n\t"
    "buffer_load_dwordx4 v[84:87], v4, s[20:23], s51 offen offset:2048 sc1\n\t"
    "buffer_load_dwordx4 v[28:31], v4, s[20:23], s51 offen offset:3072 sc1\n\t"
    "v_exp_f32 v42, v42\n\t"
    "v_exp_f32 v43, v43\n\t"
    "v_exp_f32 v44, v44\n\t"
    "v_exp_f32 v45, v45\n\t"
    "v_mov_b32 v26, v12\n\t"
    "v_mov_b32 v27, v13\n\t"
    "ds_write_b128 v3, v[16:19] offset:32768\n\t"
    "v_exp_f32 v46, v46\n\t"
    "ds_write_b128 v3, v[20:23] offset:33792\n\t"
    "v_exp_f32 v47, v47\n\t"
    "ds_write_b128 v3, v[24:27] offset:34816\n\t"
    "v_exp_f32 v48, v48\n\t"
    "s_waitcnt lgkmcnt(3)\n\t"
    "v_exp_f32 v49, v49\n\t"
    "v_smfmac_f32_32x32x32_bf16 v[116:131], v[112:115], v[40:47], v15\n\t"
    "s_mov_b64 exec, s[66:67]\n\t"
    "ds_read_b128 v[108:111], v5 offset:28672\n\t"
    "ds_read_b128 v[112:115], v5 offset:29184\n\t"
    "s_mov_b64 exec, -1\n\t"
    "v_exp_f32 v50, v50\n\t"
    "v_exp_f32 v51, v51\n\t"
    "v_exp_f32 v52, v52\n\t"
    "v_exp_f32 v53, v53\n\t"
    "ds_read_b128 v[24:27], v2 offset:34816\n\t"
    "v_exp_f32 v54, v54\n\t"
    "ds_read_b128 v[16:19], v2 offset:32768\n\t"
    "v_exp_f32 v55, v55\n\t"
    "ds_read_b128 v[20:23], v2 offset:33792\n\t"
    "v_exp_f32 v56, v56\n\t"
    "s_waitcnt lgkmcnt(4)\n\t"
    "v_exp_f32 v57, v57\n\t"
    "v_smfmac_f32_32x32x32_bf16 v[116:131], v[108:111], v[48:55], v15\n\t"
    "v_exp_f32 v58, v58\n\t"
    "v_exp_f32 v59, v59\n\t"
    "v_exp_f32 v60, v60\n\t"
    "v_exp_f32 v61, v61\n\t"
    "v_exp_f32 v62, v62\n\t"
    "v_exp_f32 v63, v63\n\t"
    "s_waitcnt vmcnt(8) lgkmcnt(3)\n\t"
    "s_setprio 3\n\t"
    "v_smfmac_f32_32x32x32_bf16 v[116:131], v[112:115], v[56:63], v15\n\t"
    "ds_read_b64_tr_b16 v[32:33], v10 offset:0\n\t"
    "ds_read_b64_tr_b16 v[34:35], v10 offset:256\n\t"
    "ds_read_b64_tr_b16 v[36:37], v10 offset:512\n\t"
    "ds_read_b64_tr_b16 v[38:39], v10 offset:768\n\t"
    "s_waitcnt lgkmcnt(4)\n\t"
    "v_cmp_ne_u64 vcc, v[12:13], v[26:27]\n\t"
    "s_nop 3\n\t"
    "s_cbranch_vccnz .Lfmm_xspin\n\t"
    ".Lfmm_xspun:\n\t"
    "s_nop 0\n\t"
    "v_add_f32 v124, v124, v24\n\t"
    "v_pk_add_f32 v[116:117], v[116:117], v[16:17]\n\t"
    "v_pk_add_f32 v[118:119], v[118:119], v[18:19]\n\t"
    "v_pk_add_f32 v[120:121], v[120:121], v[20:21]\n\t"
    "v_pk_add_f32 v[122:123], v[122:123], v[22:23]\n\t"
    "v_mov_b32 v26, v124\n\t"
    "v_pk_add_f32 v[100:101], v[116:117], v[118:119]\n\t"
    "v_pk_add_f32 v[102:103], v[120:121], v[122:123]\n\t"
    "v_permlane32_swap_b32 v124, v26\n\t"
    "v_pk_add_f32 v[100:101], v[100:101], v[102:103]\n\t"
    "v_rcp_f32 v26, v124\n\t"
    "v_add_f32 v100, v100, v101\n\t"
    "v_fma_f32 v100, v100, 0, v124\n\t"
    "v_subrev_u32 v100, 0x30800000, v100\n\t"
    "v_cmp_lt_u32 vcc, 0x41000000, v100\n\t"
    "v_pk_mul_f32 v[116:117], v[116:117], v[26:27] op_sel_hi:[1,0]\n\t"
    "v_pk_mul_f32 v[118:119], v[118:119], v[26:27] op_sel_hi:[1,0]\n\t"
    "v_pk_mul_f32 v[120:121], v[120:121], v[26:27] op_sel_hi:[1,0]\n\t"
    "v_pk_mul_f32 v[122:123], v[122:123], v[26:27] op_sel_hi:[1,0]\n\t"
    "v_cvt_pk_bf16_f32 v108, v116, v117\n\t"
    "v_cvt_pk_bf16_f32 v109, v118, v119\n\t"
    "v_cvt_pk_bf16_f32 v110, v120, v121\n\t"
    "v_cvt_pk_bf16_f32 v111, v122, v123\n\t"
    "s_cbranch_vccnz .Lfmm_slow\n\t"
    ".Lfmm_pub:\n\t"
    "buffer_store_dwordx4 v[108:111], v4, s[20:23], s99 offen sc1\n\t"
    "s_cmp_gt_u32 s44, 3\n\t"
    "s_cbranch_scc1 38f\n\t"
    "s_setprio 0\n\t"
    "38:\n\t"
    "v_mfma_f32_32x32x16_bf16 v[136:151], v[64:67], v[88:91], v[136:151]\n\t"
    "v_and_b32 v3, 0xffffffc0, v1\n\t"
    "v_bfe_u32 v1, v0, 4, 2\n\t"
    "v_and_b32 v2, 7, v0\n\t"
    "v_and_b32 v7, 8, v0\n\t"
    "v_mfma_f32_32x32x16_bf16 v[168:183], v[68:71], v[88:91], v[168:183]\n\t"
    "v_lshlrev_b32 v9, 8, v7\n\t"
    "v_bfrev_b32 v7, v1\n\t"
    "v_lshrrev_b32 v7, 26, v7\n\t"
    "v_add_u32 v18, v3, v7\n\t"
    "v_mfma_f32_32x32x16_bf16 v[152:167], v[64:67], v[92:95], v[152:167]\n\t"
    "v_lshrrev_b32 v7, 1, v1\n\t"
    "v_lshlrev_b32 v3, 7, v7\n\t"
    "v_xor_b32 v3, v3, v4\n\t"
    "v_lshl_add_u32 v8, s44, 13, v3\n\t"
    "v_mfma_f32_32x32x16_bf16 v[184:199], v[68:71], v[92:95], v[184:199]\n\t"
    "v_lshl_or_b32 v9, v7, 13, v9\n\t"
    "v_and_b32 v3, 1, v1\n\t"
    "v_lshl_or_b32 v9, v3, 9, v9\n\t"
    "v_xor_b32 v7, s49, v3\n\t"
    "v_mfma_f32_32x32x16_bf16 v[136:151], v[72:75], v[232:235], v[136:151]\n\t"
    "v_lshl_or_b32 v7, v7, 3, v2\n\t"
    "v_lshl_or_b32 v9, v7, 4, v9\n\t"
    "s_and_b32 s62, s44, 4\n\t"
    "v_lshl_or_b32 v9, s62, 10, v9\n\t"
    "v_mfma_f32_32x32x16_bf16 v[168:183], v[76:79], v[232:235], v[168:183]\n\t"
    "v_and_b32 v2, 15, v0\n\t"
    "v_lshlrev_b32 v3, 3, v3\n\t"
    "v_xor_b32 v3, v3, v2\n\t"
    "v_lshlrev_b32 v7, 4, v2\n\t"
    "v_mfma_f32_32x32x16_bf16 v[152:167], v[72:75], v[236:239], v[152:167]\n\t"
    "v_and_b32 v7, 16, v7\n\t"
    "v_mov_b32 v1, 0x3f80\n\t"
    "v_lshlrev_b32 v7, v7, v1\n\t"
    "v_lshrrev_b32 v3, 1, v3\n\t"
    "v_mfma_f32_32x32x16_bf16 v[184:199], v[76:79], v[236:239], v[184:199]\n\t"
    "v_cmp_eq_u32 vcc, 0, v3\n\t"
    "v_cmp_eq_u32 s[60:61], 1, v3\n\t"
    "v_cmp_eq_u32 s[62:63], 2, v3\n\t"
    "v_cmp_eq_u32 s[64:65], 3, v3\n\t"
    "s_waitcnt vmcnt(6)\n\t"
    "ds_read_b64_tr_b16 v[40:41], v10 offset:16384\n\t"
    "ds_read_b64_tr_b16 v[42:43], v10 offset:16640\n\t"
    "ds_read_b64_tr_b16 v[44:45], v10 offset:16896\n\t"
    "ds_read_b64_tr_b16 v[46:47], v10 offset:17152\n\t"
    "s_waitcnt lgkmcnt(4)\n\t"
    "v_mfma_f32_32x32x16_bf16 v[136:151], v[32:35], v[240:243], v[136:151]\n\t"
    "v_cndmask_b32 v100, 0, v7, vcc\n\t"
    "v_cndmask_b32 v101, 0, v7, s[60:61]\n\t"
    "v_cndmask_b32 v102, 0, v7, s[62:63]\n\t"
    "v_cndmask_b32 v103, 0, v7, s[64:65]\n\t"
    "v_mfma_f32_32x32x16_bf16 v[168:183], v[36:39], v[240:243], v[168:183]\n\t"
    "s_add_u32 s68, s57, 4096\n\t"
    "v_mfma_f32_32x32x16_bf16 v[152:167], v[32:35], v[244:247], v[152:167]\n\t"
    "v_mfma_f32_32x32x16_bf16 v[184:199], v[36:39], v[244:247], v[184:199]\n\t"
    "s_waitcnt lgkmcnt(0)\n\t"
    "v_mfma_f32_32x32x16_bf16 v[136:151], v[40:43], v[248:251], v[136:151]\n\t"
    "v_mfma_f32_32x32x16_bf16 v[168:183], v[44:47], v[248:251], v[168:183]\n\t"
    "v_mfma_f32_32x32x16_bf16 v[152:167], v[40:43], v[252:255], v[152:167]\n\t"
    "v_mfma_f32_32x32x16_bf16 v[184:199], v[44:47], v[252:255], v[184:199]\n\t"
    "s_cmp_gt_u32 s44, 3\n\t"
    "s_movk_i32 s69, 2\n\t"
    "s_cbranch_scc1 39f\n\t"
    "s_branch 36f\n\t"
    "s_waitcnt vmcnt(1)\n\t"
    "40:\n\t"
    "v_max3_u32 v20, v132, v133, v134\n\t"
    "v_max_u32 v20, v20, v135\n\t"
    "v_max3_u32 v20, v20, v80, v81\n\t"
    "v_max3_u32 v20, v20, v82, v83\n\t"
    "v_max3_u32 v20, v20, v84, v85\n\t"
    "v_max3_u32 v20, v20, v86, v87\n\t"
    "v_max3_u32 v20, v20, v28, v29\n\t"
    "v_max3_u32 v20, v20, v30, v31\n\t"
    "v_cmp_eq_u32 vcc, -1, v20\n\t"
    "s_nop 0\n\t"
    "s_cmp_lg_u64 vcc, 0\n\t"
    "s_cbranch_scc0 41f\n\t"
    "36:\n\t"
    "s_sub_u32 s69, s69, 1\n\t"
    "s_cbranch_scc1 42f\n\t"
    "s_waitcnt vmcnt(0)\n\t"
    "s_sleep 4\n\t"
    "buffer_load_dwordx4 v[132:135], v4, s[20:23], s57 offen offset:0 sc1\n\t"
    "buffer_load_dwordx4 v[80:83], v4, s[20:23], s57 offen offset:1024 sc1\n\t"
    "buffer_load_dwordx4 v[84:87], v4, s[20:23], s57 offen offset:2048 sc1\n\t"
    "buffer_load_dwordx4 v[28:31], v4, s[20:23], s57 offen offset:3072 sc1\n\t"
    "s_waitcnt vmcnt(0)\n\t"
    "s_branch 40b\n\t"
    "42:\n\t"
    "s_waitcnt vmcnt(0)\n\t"
    "s_barrier\n\t"
    "buffer_load_dwordx4 v[132:135], v4, s[20:23], s57 offen offset:0 sc1\n\t"
    "buffer_load_dwordx4 v[80:83], v4, s[20:23], s57 offen offset:1024 sc1\n\t"
    "buffer_load_dwordx4 v[84:87], v4, s[20:23], s57 offen offset:2048 sc1\n\t"
    "buffer_load_dwordx4 v[28:31], v4, s[20:23], s57 offen offset:3072 sc1\n\t"
    "buffer_load_dwordx4 v[232:235], v4, s[20:23], s68 offen offset:0 sc1\n\t"
    "buffer_load_dwordx4 v[236:239], v4, s[20:23], s68 offen offset:1024 sc1\n\t"
    "buffer_load_dwordx4 v[240:243], v4, s[20:23], s68 offen offset:2048 sc1\n\t"
    "buffer_load_dwordx4 v[244:247], v4, s[20:23], s68 offen offset:3072 sc1\n\t"
    "s_waitcnt vmcnt(4)\n\t"
    "s_branch 35f\n\t"
    "39:\n\t"
    "s_waitcnt vmcnt(1)\n\t"
    "v_max3_u32 v20, v132, v133, v134\n\t"
    "v_max_u32 v20, v20, v135\n\t"
    "v_max3_u32 v20, v20, v80, v81\n\t"
    "v_max3_u32 v20, v20, v82, v83\n\t"
    "v_max3_u32 v20, v20, v84, v85\n\t"
    "v_max3_u32 v20, v20, v86, v87\n\t"
    "v_max3_u32 v20, v20, v28, v29\n\t"
    "v_max3_u32 v20, v20, v30, v31\n\t"
    "v_cmp_eq_u32 vcc, -1, v20\n\t"
    "s_nop 0\n\t"
    "s_cmp_lg_u64 vcc, 0\n\t"
    "s_cbranch_scc1 42b\n\t"
    "34:\n\t"
    "s_waitcnt vmcnt(0)\n\t"
    "s_barrier\n\t"
    "buffer_load_dwordx4 v[232:235], v4, s[20:23], s68 offen offset:0 sc1\n\t"
    "buffer_load_dwordx4 v[236:239], v4, s[20:23], s68 offen offset:1024 sc1\n\t"
    "buffer_load_dwordx4 v[240:243], v4, s[20:23], s68 offen offset:2048 sc1\n\t"
    "buffer_load_dwordx4 v[244:247], v4, s[20:23], s68 offen offset:3072 sc1\n\t"
    "s_branch 44f\n\t"
    "35:\n\t"
    "s_mov_b32 s69, 0\n\t"
    "v_max3_u32 v20, v132, v133, v134\n\t"
    "v_max_u32 v20, v20, v135\n\t"
    "v_max3_u32 v20, v20, v80, v81\n\t"
    "v_max3_u32 v20, v20, v82, v83\n\t"
    "v_max3_u32 v20, v20, v84, v85\n\t"
    "v_max3_u32 v20, v20, v86, v87\n\t"
    "v_max3_u32 v20, v20, v28, v29\n\t"
    "v_max3_u32 v20, v20, v30, v31\n\t"
    "v_cmp_eq_u32 vcc, -1, v20\n\t"
    "s_nop 0\n\t"
    "s_cmp_lg_u64 vcc, 0\n\t"
    "s_cbranch_scc0 44f\n\t"
    "43:\n\t"
    "buffer_load_dwordx4 v[132:135], v4, s[20:23], s57 offen offset:0 sc1\n\t"
    "buffer_load_dwordx4 v[80:83], v4, s[20:23], s57 offen offset:1024 sc1\n\t"
    "buffer_load_dwordx4 v[84:87], v4, s[20:23], s57 offen offset:2048 sc1\n\t"
    "buffer_load_dwordx4 v[28:31], v4, s[20:23], s57 offen offset:3072 sc1\n\t"
    "s_add_u32 s69, s69, 1\n\t"
    "s_cmp_gt_u32 s69, 0x40000\n\t"
    "s_cbranch_scc1 44f\n\t"
    "s_waitcnt vmcnt(3)\n\t"
    "v_max3_u32 v20, v132, v133, v134\n\t"
    "v_max_u32 v20, v20, v135\n\t"
    "s_waitcnt vmcnt(2)\n\t"
    "v_max3_u32 v20, v20, v80, v81\n\t"
    "v_max3_u32 v20, v20, v82, v83\n\t"
    "s_waitcnt vmcnt(1)\n\t"
    "v_max3_u32 v20, v20, v84, v85\n\t"
    "v_max3_u32 v20, v20, v86, v87\n\t"
    "s_waitcnt vmcnt(0)\n\t"
    "v_max3_u32 v20, v20, v28, v29\n\t"
    "v_max3_u32 v20, v20, v30, v31\n\t"
    "v_cmp_eq_u32 vcc, -1, v20\n\t"
    "s_nop 0\n\t"
    "s_cmp_lg_u64 vcc, 0\n\t"
    "s_cbranch_scc1 43b\n\t"
    "44:\n\t"
    "v_mfma_f32_32x32x16_bf16 v[136:151], v[200:203], v[132:135], v[136:151]\n\t"
    "v_mfma_f32_32x32x16_bf16 v[136:151], v[208:211], v[80:83], v[136:151]\n\t"
    "v_mfma_f32_32x32x16_bf16 v[136:151], v[216:219], v[84:87], v[136:151]\n\t"
    "v_mfma_f32_32x32x16_bf16 v[136:151], v[224:227], v[28:31], v[136:151]\n\t"
    "v_mfma_f32_32x32x16_bf16 v[168:183], v[204:207], v[132:135], v[168:183]\n\t"
    "s_nop 4\n\t"
    "v_mfma_f32_32x32x16_bf16 v[168:183], v[212:215], v[80:83], v[168:183]\n\t"
    "s_nop 4\n\t"
    "v_mfma_f32_32x32x16_bf16 v[168:183], v[220:223], v[84:87], v[168:183]\n\t"
    "v_cvt_pk_bf16_f32 v116, v136, v137\n\t"
    "v_cvt_pk_bf16_f32 v117, v138, v139\n\t"
    "v_cvt_pk_bf16_f32 v118, v140, v141\n\t"
    "v_cvt_pk_bf16_f32 v119, v142, v143\n\t"
    "ds_write_b128 v8, v[116:119] offset:0\n\t"
    "v_mfma_f32_32x32x16_bf16 v[168:183], v[228:231], v[28:31], v[168:183]\n\t"
    "v_cvt_pk_bf16_f32 v120, v144, v145\n\t"
    "v_cvt_pk_bf16_f32 v121, v146, v147\n\t"
    "v_cvt_pk_bf16_f32 v122, v148, v149\n\t"
    "v_cvt_pk_bf16_f32 v123, v150, v151\n\t"
    "ds_write_b128 v8, v[120:123] offset:1024\n\t"
    "s_nop 6\n\t"
    "v_cvt_pk_bf16_f32 v124, v168, v169\n\t"
    "v_cvt_pk_bf16_f32 v125, v170, v171\n\t"
    "v_cvt_pk_bf16_f32 v126, v172, v173\n\t"
    "v_cvt_pk_bf16_f32 v127, v174, v175\n\t"
    "ds_write_b128 v8, v[124:127] offset:2048\n\t"
    "v_cvt_pk_bf16_f32 v128, v176, v177\n\t"
    "v_cvt_pk_bf16_f32 v129, v178, v179\n\t"
    "v_cvt_pk_bf16_f32 v130, v180, v181\n\t"
    "v_cvt_pk_bf16_f32 v131, v182, v183\n\t"
    "ds_write_b128 v8, v[128:131] offset:3072\n\t"
    "s_branch 45f\n\t"
    "41:\n\t"
    "v_mfma_f32_32x32x16_bf16 v[136:151], v[200:203], v[132:135], v[136:151]\n\t"
    "v_mfma_f32_32x32x16_bf16 v[136:151], v[208:211], v[80:83], v[136:151]\n\t"
    "v_mfma_f32_32x32x16_bf16 v[136:151], v[216:219], v[84:87], v[136:151]\n\t"
    "v_mfma_f32_32x32x16_bf16 v[136:151], v[224:227], v[28:31], v[136:151]\n\t"
    "v_mfma_f32_32x32x16_bf16 v[168:183], v[204:207], v[132:135], v[168:183]\n\t"
    "s_nop 4\n\t"
    "v_mfma_f32_32x32x16_bf16 v[168:183], v[212:215], v[80:83], v[168:183]\n\t"
    "s_nop 4\n\t"
    "v_mfma_f32_32x32x16_bf16 v[168:183], v[220:223], v[84:87], v[168:183]\n\t"
    "v_cvt_pk_bf16_f32 v116, v136, v137\n\t"
    "v_cvt_pk_bf16_f32 v117, v138, v139\n\t"
    "v_cvt_pk_bf16_f32 v118, v140, v141\n\t"
    "v_cvt_pk_bf16_f32 v119, v142, v143\n\t"
    "v_mfma_f32_32x32x16_bf16 v[168:183], v[228:231], v[28:31], v[168:183]\n\t"
    "v_cvt_pk_bf16_f32 v120, v144, v145\n\t"
    "v_cvt_pk_bf16_f32 v121, v146, v147\n\t"
    "v_cvt_pk_bf16_f32 v122, v148, v149\n\t"
    "v_cvt_pk_bf16_f32 v123, v150, v151\n\t"
    "s_nop 7\n\t"
    "v_cvt_pk_bf16_f32 v124, v168, v169\n\t"
    "v_cvt_pk_bf16_f32 v125, v170, v171\n\t"
    "v_cvt_pk_bf16_f32 v126, v172, v173\n\t"
    "v_cvt_pk_bf16_f32 v127, v174, v175\n\t"
    "v_cvt_pk_bf16_f32 v128, v176, v177\n\t"
    "v_cvt_pk_bf16_f32 v129, v178, v179\n\t"
    "v_cvt_pk_bf16_f32 v130, v180, v181\n\t"
    "v_cvt_pk_bf16_f32 v131, v182, v183\n\t"
    "s_waitcnt vmcnt(0)\n\t"
    "s_barrier\n\t"
    "buffer_load_dwordx4 v[232:235], v4, s[20:23], s68 offen offset:0 sc1\n\t"
    "buffer_load_dwordx4 v[236:239], v4, s[20:23], s68 offen offset:1024 sc1\n\t"
    "buffer_load_dwordx4 v[240:243], v4, s[20:23], s68 offen offset:2048 sc1\n\t"
    "buffer_load_dwordx4 v[244:247], v4, s[20:23], s68 offen offset:3072 sc1\n\t"
    "ds_write_b128 v8, v[116:119] offset:0\n\t"
    "ds_write_b128 v8, v[120:123] offset:1024\n\t"
    "ds_write_b128 v8, v[124:127] offset:2048\n\t"
    "ds_write_b128 v8, v[128:131] offset:3072\n\t"
    "45:\n\t"
    "s_waitcnt vmcnt(4)\n\t"
    "v_lshlrev_b32 v48, 16, v104\n\t"
    "v_and_b32 v49, 0xffff0000, v104\n\t"
    "v_lshlrev_b32 v50, 16, v105\n\t"
    "v_and_b32 v51, 0xffff0000, v105\n\t"
    "v_lshlrev_b32 v52, 16, v106\n\t"
    "v_and_b32 v53, 0xffff0000, v106\n\t"
    "v_lshlrev_b32 v54, 16, v107\n\t"
    "v_and_b32 v55, 0xffff0000, v107\n\t"
    "s_mov_b32 s69, 0\n\t"
    "s_setprio 0\n\t"
    "s_branch 47f\n\t"
    "46:\n\t"
    "buffer_load_dwordx4 v[232:235], v4, s[20:23], s68 offen offset:0 sc1\n\t"
    "buffer_load_dwordx4 v[236:239], v4, s[20:23], s68 offen offset:1024 sc1\n\t"
    "buffer_load_dwordx4 v[240:243], v4, s[20:23], s68 offen offset:2048 sc1\n\t"
    "buffer_load_dwordx4 v[244:247], v4, s[20:23], s68 offen offset:3072 sc1\n\t"
    "s_add_u32 s69, s69, 1\n\t"
    "s_cmp_gt_u32 s69, 0x40000\n\t"
    "s_cbranch_scc1 48f\n\t"
    "47:\n\t"
    "s_waitcnt vmcnt(3)\n\t"
    "v_mfma_f32_32x32x16_bf16 v[136:151], v[200:203], v[232:235], v[152:167]\n\t"
    "v_max3_u32 v20, v232, v233, v234\n\t"
    "v_max_u32 v20, v20, v235\n\t"
    "v_mfma_f32_32x32x16_bf16 v[168:183], v[204:207], v[232:235], v[184:199]\n\t"
    "s_waitcnt vmcnt(2)\n\t"
    "v_mfma_f32_32x32x16_bf16 v[136:151], v[208:211], v[236:239], v[136:151]\n\t"
    "v_max3_u32 v20, v20, v236, v237\n\t"
    "v_max3_u32 v20, v20, v238, v239\n\t"
    "v_mfma_f32_32x32x16_bf16 v[168:183], v[212:215], v[236:239], v[168:183]\n\t"
    "s_waitcnt vmcnt(1)\n\t"
    "v_mfma_f32_32x32x16_bf16 v[136:151], v[216:219], v[240:243], v[136:151]\n\t"
    "v_max3_u32 v20, v20, v240, v241\n\t"
    "v_max3_u32 v20, v20, v242, v243\n\t"
    "s_waitcnt vmcnt(0)\n\t"
    "v_mfma_f32_32x32x16_bf16 v[136:151], v[224:227], v[244:247], v[136:151]\n\t"
    "v_max3_u32 v20, v20, v244, v245\n\t"
    "v_max3_u32 v20, v20, v246, v247\n\t"
    "v_cmp_eq_u32 vcc, -1, v20\n\t"
    "v_mfma_f32_32x32x16_bf16 v[168:183], v[220:223], v[240:243], v[168:183]\n\t"
    "v_mfma_f32_32x32x16_bf16 v[168:183], v[228:231], v[244:247], v[168:183]\n\t"
    "s_cbranch_vccnz 46b\n\t"
    "48:\n\t"
    "s_nop 5\n\t"
    "v_cvt_pk_bf16_f32 v116, v136, v137\n\t"
    "v_cvt_pk_bf16_f32 v117, v138, v139\n\t"
    "v_cvt_pk_bf16_f32 v118, v140, v141\n\t"
    "v_cvt_pk_bf16_f32 v119, v142, v143\n\t"
    "ds_write_b128 v8, v[116:119] offset:4096\n\t"
    "v_cvt_pk_bf16_f32 v120, v144, v145\n\t"
    "v_cvt_pk_bf16_f32 v121, v146, v147\n\t"
    "v_cvt_pk_bf16_f32 v122, v148, v149\n\t"
    "v_cvt_pk_bf16_f32 v123, v150, v151\n\t"
    "ds_write_b128 v8, v[120:123] offset:5120\n\t"
    "v_cvt_pk_bf16_f32 v124, v168, v169\n\t"
    "v_cvt_pk_bf16_f32 v125, v170, v171\n\t"
    "v_cvt_pk_bf16_f32 v126, v172, v173\n\t"
    "v_cvt_pk_bf16_f32 v127, v174, v175\n\t"
    "ds_write_b128 v8, v[124:127] offset:6144\n\t"
    "v_cvt_pk_bf16_f32 v128, v176, v177\n\t"
    "v_cvt_pk_bf16_f32 v129, v178, v179\n\t"
    "v_cvt_pk_bf16_f32 v130, v180, v181\n\t"
    "v_cvt_pk_bf16_f32 v131, v182, v183\n\t"
    "ds_write_b128 v8, v[128:131] offset:7168\n\t"
    "s_waitcnt lgkmcnt(0)\n\t"
    "s_barrier\n\t"
    "ds_read_b128 v[32:35], v9 offset:0\n\t"
    "ds_read_b128 v[36:39], v9 offset:1024\n\t"
    "ds_read_b128 v[40:43], v9 offset:16384\n\t"
    "ds_read_b128 v[44:47], v9 offset:17408\n\t"
    "ds_read_b128 v[64:67], v9 offset:32768\n\t"
    "ds_read_b128 v[68:71], v9 offset:33792\n\t"
    "ds_read_b128 v[72:75], v9 offset:49152\n\t"
    "ds_read_b128 v[76:79], v9 offset:50176\n\t"
    "s_waitcnt lgkmcnt(7)\n\t"
    "v_mfma_f32_16x16x32_bf16 v[56:59], v[100:103], v[32:35], 0\n\t"
    "s_waitcnt lgkmcnt(6)\n\t"
    "v_mfma_f32_16x16x32_bf16 v[60:63], v[100:103], v[36:39], 0\n\t"
    "s_waitcnt lgkmcnt(5)\n\t"
    "v_mfma_f32_16x16x32_bf16 v[56:59], v[100:103], v[40:43], v[56:59]\n\t"
    "s_waitcnt lgkmcnt(4)\n\t"
    "v_mfma_f32_16x16x32_bf16 v[60:63], v[100:103], v[44:47], v[60:63]\n\t"
    "s_waitcnt lgkmcnt(3)\n\t"
    "v_mfma_f32_16x16x32_bf16 v[56:59], v[100:103], v[64:67], v[56:59]\n\t"
    "s_waitcnt lgkmcnt(2)\n\t"
    "v_mfma_f32_16x16x32_bf16 v[60:63], v[100:103], v[68:71], v[60:63]\n\t"
    "s_waitcnt lgkmcnt(1)\n\t"
    "v_mfma_f32_16x16x32_bf16 v[56:59], v[100:103], v[72:75], v[56:59]\n\t"
    "s_waitcnt lgkmcnt(0)\n\t"
    "v_mfma_f32_16x16x32_bf16 v[60:63], v[100:103], v[76:79], v[60:63]\n\t"
    "s_nop 5\n\t"
    "v_fmac_f32 v48, 0x3fb17218, v56\n\t"
    "v_fmac_f32 v49, 0x3fb17218, v57\n\t"
    "v_fmac_f32 v50, 0x3fb17218, v58\n\t"
    "v_fmac_f32 v51, 0x3fb17218, v59\n\t"
    "v_fmac_f32 v52, 0x3fb17218, v60\n\t"
    "v_fmac_f32 v53, 0x3fb17218, v61\n\t"
    "v_fmac_f32 v54, 0x3fb17218, v62\n\t"
    "v_fmac_f32 v55, 0x3fb17218, v63\n\t"
    "v_cvt_pk_bf16_f32 v56, v48, v49\n\t"
    "v_cvt_pk_bf16_f32 v57, v50, v51\n\t"
    "v_cvt_pk_bf16_f32 v58, v52, v53\n\t"
    "v_cvt_pk_bf16_f32 v59, v54, v55\n\t"
    "s_nop 0\n\t"
    "v_permlane16_swap_b32 v56, v58\n\t"
    "v_permlane16_swap_b32 v57, v59\n\t"
    "buffer_store_dwordx4 v[56:59], v18, s[40:43], 0 offen sc1\n\t"
    "s_branch .Lfmm_end\n\t"
    ".Lfmm_spin:\n\t"
    "s_mov_b32 s64, 0\n\t"
    "1:\n\t"
    "s_sleep 2\n\t"
    "buffer_load_dwordx2 v[132:133], v7, s[20:23], 0 offen sc1\n\t"
    "s_add_u32 s64, s64, 1\n\t"
    "s_waitcnt vmcnt(0)\n\t"
    "v_cmp_eq_u64 vcc, v[12:13], v[132:133]\n\t"
    "s_cmp_gt_u32 s64, 0x40000\n\t"
    "s_cbranch_scc1 .Lfmm_spun\n\t"
    "s_cmp_eq_u64 vcc, -1\n\t"
    "s_cbranch_scc0 1b\n\t"
    "s_branch .Lfmm_spun\n\t"
    ".Lfmm_xspin:\n\t"
    "s_mov_b32 s64, 0\n\t"
    "1:\n\t"
    "ds_read_b128 v[24:27], v2 offset:34816\n\t"
    "ds_read_b128 v[16:19], v2 offset:32768\n\t"
    "ds_read_b128 v[20:23], v2 offset:33792\n\t"
    "s_add_u32 s64, s64, 1\n\t"
    "s_waitcnt lgkmcnt(0)\n\t"
    "v_cmp_ne_u64 vcc, v[12:13], v[26:27]\n\t"
    "s_cmp_gt_u32 s64, 0x400000\n\t"
    "s_cbranch_scc1 .Lfmm_xspun\n\t"
    "s_nop 4\n\t"
    "s_cbranch_vccnz 1b\n\t"
    "s_branch .Lfmm_xspun\n\t"
    ".Lfmm_slow:\n\t"
    "v_mov_b32 v16, 0xff800000\n\t"
    "s_mov_b32 s60, 0\n\t"
    "60:\n\t"
    "buffer_load_dwordx4 v[100:103], v4, s[20:23], s60 offen sc1\n\t"
    "s_add_u32 s60, s60, 0x800\n\t"
    "s_waitcnt vmcnt(0)\n\t"
    "v_mfma_f32_32x32x16_bf16 v[48:63], v[100:103], v[96:99], 0\n\t"
    "s_nop 11\n\t"
    "v_max3_f32 v16, v16, v48, v49\n\t"
    "v_max3_f32 v16, v16, v50, v51\n\t"
    "v_max3_f32 v16, v16, v52, v53\n\t"
    "v_max3_f32 v16, v16, v54, v55\n\t"
    "v_max3_f32 v16, v16, v56, v57\n\t"
    "v_max3_f32 v16, v16, v58, v59\n\t"
    "v_max3_f32 v16, v16, v60, v61\n\t"
    "v_max3_f32 v16, v16, v62, v63\n\t"
    "s_cmp_lt_u32 s60, 0x20000\n\t"
    "s_cbranch_scc1 60b\n\t"
    "v_mov_b32 v2, v16\n\t"
    "s_nop 1\n\t"
    "v_permlane32_swap_b32 v16, v2\n\t"
    "s_nop 1\n\t"
    "v_max_f32 v16, v16, v2\n\t"
    "v_and_b32 v3, 0x7ff, v5\n\t"
    "s_mov_b32 s60, 0\n\t"
    "v_mov_b32 v116, 0\n\t"
    "v_mov_b32 v117, 0\n\t"
    "v_mov_b32 v118, 0\n\t"
    "v_mov_b32 v119, 0\n\t"
    "v_mov_b32 v120, 0\n\t"
    "v_mov_b32 v121, 0\n\t"
    "v_mov_b32 v122, 0\n\t"
    "v_mov_b32 v123, 0\n\t"
    "v_mov_b32 v124, 0\n\t"
    "v_mov_b32 v125, 0\n\t"
    "v_mov_b32 v126, 0\n\t"
    "v_mov_b32 v127, 0\n\t"
    "v_mov_b32 v128, 0\n\t"
    "v_mov_b32 v129, 0\n\t"
    "v_mov_b32 v130, 0\n\t"
    "v_mov_b32 v131, 0\n\t"
    "v_mov_b32 v108, v11\n\t"
    "v_mov_b32 v109, v11\n\t"
    "v_mov_b32 v110, v11\n\t"
    "v_mov_b32 v111, v11\n\t"
    "v_mov_b32 v112, v11\n\t"
    "v_mov_b32 v113, v11\n\t"
    "v_mov_b32 v114, v11\n\t"
    "v_mov_b32 v115, v11\n\t"
    "61:\n\t"
    "buffer_load_dwordx4 v[100:103], v4, s[20:23], s60 offen sc1\n\t"
    "s_mov_b64 exec, s[66:67]\n\t"
    "buffer_load_dwordx4 v[108:111], v3, s[20:23], s60 offen sc1\n\t"
    "buffer_load_dwordx4 v[112:115], v3, s[20:23], s60 offen offset:512 sc1\n\t"
    "s_mov_b64 exec, -1\n\t"
    "s_add_u32 s60, s60, 0x800\n\t"
    "s_waitcnt vmcnt(0)\n\t"
    "v_mfma_f32_32x32x16_bf16 v[48:63], v[100:103], v[96:99], 0\n\t"
    "s_nop 11\n\t"
    "v_sub_f32 v48, v48, v16\n\t"
    "v_sub_f32 v49, v49, v16\n\t"
    "v_sub_f32 v50, v50, v16\n\t"
    "v_sub_f32 v51, v51, v16\n\t"
    "v_sub_f32 v52, v52, v16\n\t"
    "v_sub_f32 v53, v53, v16\n\t"
    "v_sub_f32 v54, v54, v16\n\t"
    "v_sub_f32 v55, v55, v16\n\t"
    "v_sub_f32 v56, v56, v16\n\t"
    "v_sub_f32 v57, v57, v16\n\t"
    "v_sub_f32 v58, v58, v16\n\t"
    "v_sub_f32 v59, v59, v16\n\t"
    "v_sub_f32 v60, v60, v16\n\t"
    "v_sub_f32 v61, v61, v16\n\t"
    "v_sub_f32 v62, v62, v16\n\t"
    "v_sub_f32 v63, v63, v16\n\t"
    "v_exp_f32 v48, v48\n\t"
    "v_exp_f32 v49, v49\n\t"
    "v_exp_f32 v50, v50\n\t"
    "v_exp_f32 v51, v51\n\t"
    "v_exp_f32 v52, v52\n\t"
    "v_exp_f32 v53, v53\n\t"
    "v_exp_f32 v54, v54\n\t"
    "v_exp_f32 v55, v55\n\t"
    "v_exp_f32 v56, v56\n\t"
    "v_exp_f32 v57, v57\n\t"
    "v_exp_f32 v58, v58\n\t"
    "v_exp_f32 v59, v59\n\t"
    "v_exp_f32 v60, v60\n\t"
    "v_exp_f32 v61, v61\n\t"
    "v_exp_f32 v62, v62\n\t"
    "v_exp_f32 v63, v63\n\t"
    "s_nop 1\n\t"
    "v_smfmac_f32_32x32x32_bf16 v[116:131], v[108:111], v[48:55], v15\n\t"
    "v_smfmac_f32_32x32x32_bf16 v[116:131], v[112:115], v[56:63], v15\n\t"
    "s_cmp_lt_u32 s60, 0x20000\n\t"
    "s_cbranch_scc1 61b\n\t"
    "s_nop 11\n\t"
    "v_mov_b32 v26, v124\n\t"
    "s_nop 1\n\t"
    "v_permlane32_swap_b32 v124, v26\n\t"
    "s_nop 1\n\t"
    "v_rcp_f32 v26, v124\n\t"
    "s_nop 0\n\t"
    "v_pk_mul_f32 v[116:117], v[116:117], v[26:27] op_sel_hi:[1,0]\n\t"
    "v_pk_mul_f32 v[118:119], v[118:119], v[26:27] op_sel_hi:[1,0]\n\t"
    "v_pk_mul_f32 v[120:121], v[120:121], v[26:27] op_sel_hi:[1,0]\n\t"
    "v_pk_mul_f32 v[122:123], v[122:123], v[26:27] op_sel_hi:[1,0]\n\t"
    "v_cvt_pk_bf16_f32 v108, v116, v117\n\t"
    "v_cvt_pk_bf16_f32 v109, v118, v119\n\t"
    "v_cvt_pk_bf16_f32 v110, v120, v121\n\t"
    "v_cvt_pk_bf16_f32 v111, v122, v123\n\t"
    "v_min_u32 v108, -2, v108\n\t"
    "v_min_u32 v109, -2, v109\n\t"
    "v_min_u32 v110, -2, v110\n\t"
    "v_min_u32 v111, -2, v111\n\t"
    "s_branch .Lfmm_pub\n\t"
    ".Lfmm_end:\n\t"
    : "+{s[0:1]}"(karg)
    : "{s2}"(blockIdx.x), "{v0}"(threadIdx.x), "i"((__attribute__((address_space(3))) char*)smem)
    : "v1", "v2", "v3", "v4", "v5", "v6", "v7", "v8", "v9", "v10", "v11", "v12", "v13", "v14", "v15", "v16", "v17", "v18", "v19", "v20", "v21", "v22", "v23", "v24", "v25", "v26", "v27", "v28", "v29", "v30", "v31", "v32", "v33", "v34", "v35", "v36", "v37", "v38", "v39", "v40", "v41", "v42", "v43", "v44", "v45", "v46", "v47", "v48", "v49", "v50", "v51", "v52", "v53", "v54", "v55", "v56", "v57", "v58", "v59", "v60", "v61", "v62", "v63", "v64", "v65", "v66", "v67", "v68", "v69", "v70", "v71", "v72", "v73", "v74", "v75", "v76", "v77", "v78", "v79", "v80", "v81", "v82", "v83", "v84", "v85", "v86", "v87", "v88", "v89", "v90", "v91", "v92", "v93", "v94", "v95", "v96", "v97", "v98", "v99", "v100", "v101", "v102", "v103", "v104", "v105", "v106", "v107", "v108", "v109", "v110", "v111", "v112", "v113", "v114", "v115", "v116", "v117", "v118", "v119", "v120", "v121", "v122", "v123", "v124", "v125", "v126", "v127", "v128", "v129", "v130", "v131", "v132", "v133", "v134", "v135", "v136", "v137", "v138", "v139", "v140", "v141", "v142", "v143", "v144", "v145", "v146", "v147", "v148", "v149", "v150", "v151", "v152", "v153", "v154", "v155", "v156", "v157", "v158", "v159", "v160", "v161", "v162", "v163", "v164", "v165", "v166", "v167", "v168", "v169", "v170", "v171", "v172", "v173", "v174", "v175", "v176", "v177", "v178", "v179", "v180", "v181", "v182", "v183", "v184", "v185", "v186", "v187", "v188", "v189", "v190", "v191", "v192", "v193", "v194", "v195", "v196", "v197", "v198", "v199", "v200", "v201", "v202", "v203", "v204", "v205", "v206", "v207", "v208", "v209", "v210", "v211", "v212", "v213", "v214", "v215", "v216", "v217", "v218", "v219", "v220", "v221", "v222", "v223", "v224", "v225", "v226", "v227", "v228", "v229", "v230", "v231", "v232", "v233", "v234", "v235", "v236", "v237", "v238", "v239", "v240", "v241", "v242", "v243", "v244", "v245", "v246", "v247", "v248", "v249", "v250", "v251", "v252", "v253", "v254", "v255", "s3", "s4", "s5", "s6", "s7", "s8", "s9", "s10", "s11", "s12", "s13", "s14", "s15", "s16", "s17", "s18", "s19", "s20", "s21", "s22", "s23", "s24", "s25", "s26", "s27", "s28", "s29", "s30", "s31", "s33", "s34", "s35", "s36", "s37", "s38", "s39", "s40", "s41", "s42", "s43", "s44", "s45", "s46", "s47", "s48", "s49", "s50", "s51", "s52", "s53", "s54", "s55", "s56", "s57", "s58", "s59", "s60", "s61", "s62", "s63", "s64", "s65", "s66", "s67", "s68", "s69", "s92", "s93", "s94", "s95", "s96", "s97", "s98", "s99", "vcc", "scc", "memory");
}

extern "C" void kernel_launch(void* const* d_in, const int* in_sizes, int n_in, void* d_out, int out_size, void* d_ws, size_t ws_size, hipStream_t stream) {
  constexpr u32 F = 0x00020000u;
  fused_mqa_mish<<<256, 512, 0, stream>>>(Buf{d_in[0], 2048 * 512 * 2, F}, Buf{d_in[6], 512 * 1792 * 2, F}, Buf{d_in[2], 512 * 16 * 2, F}, Buf{d_in[3], 512 * 16 * 2, F},
                                          Buf{d_ws, 12u << 20, F}, Buf{d_in[7], 1792 * 2, F}, Buf{d_in[1], 512 * 512 * 2, F}, Buf{d_in[4], 512 * 512 * 2, F},
                                          Buf{d_in[8], 1792 * 512 * 2, F}, Buf{d_out, 2048 * 512 * 2, F});
}
```
